```python
import torch

def _softmax(x, axis=-1):
    return torch.softmax(x, dim=axis)
def _gelu(x):
    return torch.nn.functional.gelu(x, approximate="tanh")
def _silu(x):
    return torch.nn.functional.silu(x)
def _sigmoid(x):
    return torch.sigmoid(x)
def _layernorm(x, axis=-1, eps=1e-5):
    m = x.mean(dim=axis, keepdim=True)
    v = x.var(dim=axis, keepdim=True, unbiased=False)
    return (x - m) / torch.sqrt(v + eps)
def _scatter_add(tgt, idx, src, axis=0):
    return tgt.clone().index_add_(axis, idx, src)
def _segment_sum(src, seg, n):
    out = torch.zeros((n,) + src.shape[1:], dtype=src.dtype)
    return out.index_add_(0, seg, src)
def _pad(x, pw):
    flat = tuple(v for pair in reversed(pw) for v in pair)
    return torch.nn.functional.pad(x, flat)

kernel_name = 'standalone__mixer_channel_mlp__prompt'

def setup_inputs():

    _w = (lambda a: (lambda t: t.to(torch.bfloat16) if t.is_floating_point() else t)(torch.as_tensor(a)))
    B, S, D, F = 1, 2048, 640, 1536
    return {
        "x": _w((lambda *s: torch.randn(*s, dtype=torch.bfloat16) * 0.05)(B, S, D)),
        "w1": _w((lambda *s: torch.randn(*s, dtype=torch.bfloat16) * 0.8)(D, F) * (D ** -0.5)),
        "b1": _w((lambda *s: torch.randn(*s, dtype=torch.bfloat16) * 0.4)(F)),
        "w2": _w((lambda *s: torch.randn(*s, dtype=torch.bfloat16) * 0.05)(F, D) * (F ** -0.5)),
        "b2": _w((lambda *s: torch.randn(*s, dtype=torch.bfloat16) * 0.05)(D)),
    }

def reference(x, w1, b1, w2, b2):
    y = _layernorm(x, axis=-1)
    h = _gelu(torch.einsum("bsd,df->bsf", y, w1) + b1)
    return torch.einsum("bsf,fd->bsd", h, w2) + b2
```

```cpp
#include <hip/hip_runtime.h>
#include <stdint.h>

#define GAS __attribute__((address_space(1)))
#define LAS __attribute__((address_space(3)))
typedef unsigned v4u __attribute__((ext_vector_type(4)));
typedef unsigned v2u __attribute__((ext_vector_type(2)));
typedef float f32x4 __attribute__((ext_vector_type(4)));
typedef float f32x2 __attribute__((ext_vector_type(2)));
typedef short s16x4 __attribute__((ext_vector_type(4)));
typedef __bf16 bf16x8 __attribute__((ext_vector_type(8)));
typedef __bf16 bf16x2 __attribute__((ext_vector_type(2)));
#define DI __device__ __forceinline__

constexpr int S_ = 2048, D_ = 640, F_ = 1536;
constexpr size_t H_OFF = 65536, H_BYTES = (size_t)S_ * F_ * 2, W2F_OFF = H_OFF + H_BYTES, W2F_BYTES = (size_t)F_ * D_ * 2;
constexpr int SLOT = 32768, XPART = 8192, NSLOT = 4, RT_OFF = NSLOT * SLOT, LDS1_BYTES = RT_OFF + 8192;
constexpr int CS_OFF = RT_OFF, ST_OFF = RT_OFF + 1024;
constexpr int LDS2_BYTES = 81920;

DI f32x4 mfma(v4u a, v4u b, f32x4 c) {
  return __builtin_amdgcn_mfma_f32_16x16x32_bf16(__builtin_bit_cast(bf16x8, a), __builtin_bit_cast(bf16x8, b), c, 0, 0, 0);
}
DI void glds16(unsigned voff, const void* sbase, unsigned ldsaddr) {
  asm volatile("s_mov_b32 m0, %2\n\ts_nop 0\n\tglobal_load_lds_dwordx4 %0, %1" ::"v"(voff), "s"(sbase), "s"(ldsaddr) : "memory");
}
#define WAIT_V(n) asm volatile("s_waitcnt vmcnt(" #n ")" ::: "memory")
#define WAIT_L0() asm volatile("s_waitcnt lgkmcnt(0)" ::: "memory")
#define BAR() __builtin_amdgcn_s_barrier()
#define SCHED() __builtin_amdgcn_sched_barrier(0)
template <int N> DI void wait_v() { asm volatile("s_waitcnt vmcnt(%0)" ::"i"(N) : "memory"); }

DI v4u lds_rd128(LAS unsigned char* L, unsigned a) { return *(LAS v4u*)(L + a); }
DI v2u lds_tr(LAS unsigned char* L, unsigned a) {
  return __builtin_bit_cast(v2u, __builtin_amdgcn_ds_read_tr16_b64_v4i16((LAS s16x4*)(L + a)));
}
DI unsigned pk_bf16(float a, float b) {
  f32x2 f = {a, b};
  return __builtin_bit_cast(unsigned, __builtin_convertvector(f, bf16x2));
}
DI float bf_lo(unsigned u) { return __builtin_bit_cast(float, u << 16); }
DI float bf_hi(unsigned u) { return __builtin_bit_cast(float, u & 0xffff0000u); }
DI float gelu_tanh(float v) {
  constexpr float K1 = -2.0f * 0.7978845608028654f * 1.4426950408889634f;
  constexpr float K2 = K1 * 0.044715f;
  const float p = __builtin_fmaf(v * v, K2, K1);
  const float e = __builtin_amdgcn_exp2f(v * p);
  return v * __builtin_amdgcn_rcpf(1.0f + e);
}

struct Frags { v4u W[6]; v4u X[2]; };

__global__ void __launch_bounds__(256, 1) k_phase1(const unsigned short* __restrict__ x, const unsigned short* __restrict__ w1, const unsigned short* __restrict__ b1,
                                                 const unsigned short* __restrict__ w2, unsigned char* __restrict__ ws) {
  extern __shared__ __attribute__((aligned(16))) unsigned char smem[];
  LAS unsigned char* L = (LAS unsigned char*)smem;
  const int tid = threadIdx.x, l = tid & 63, w = __builtin_amdgcn_readfirstlane(tid >> 6);
  const int q = l >> 4, t = l & 15, qq = t >> 2, p = l & 3;
  const int wf = w & 1, wt = w >> 1;
  const int b = blockIdx.x, xcd = b & 7, idx = b >> 3;
  const int g = xcd * 4 + (idx >> 3), j = idx & 7;

  v2u b1r[6];
#pragma unroll
  for (int i = 0; i < 6; ++i) {
    const unsigned off = (unsigned)((192 * j + 96 * wf + 16 * (i ^ wt) + 4 * q) * 2);
    asm volatile("global_load_dwordx2 %0, %1, %2" : "=v"(b1r[i]) : "v"(off), "s"(b1) : "memory");
  }
  const int rb = b < 240 ? b : b - 240, rks = rb / 5, rng = rb % 5;
  {
    const unsigned char* sb = (const unsigned char*)w2 + (size_t)rks * 32 * 1280 + rng * 256;
#pragma unroll
    for (int i = 0; i < 2; ++i) glds16((unsigned)((8 * w + 4 * i + (l >> 4)) * 1280 + (l & 15) * 16), sb, (unsigned)(RT_OFF + w * 2048 + i * 1024));
  }
  unsigned voffX[2], voffW[6];
#pragma unroll
  for (int i = 0; i < 2; ++i) {
    const int row = 16 * w + 8 * i + (l >> 3);
    voffX[i] = (unsigned)(row * 1280 + (((l & 7) ^ ((row >> 1) & 7)) * 16));
  }
#pragma unroll
  for (int i = 0; i < 6; ++i) {
    const int pos = i * 64 + l, rr = pos / 24, c1 = pos % 24, r = 16 * w + rr;
    const int gr = ((r >> 1) & 1) | (((r >> 3) & 1) << 1);
    voffW[i] = (unsigned)(r * 3072 + ((c1 ^ (gr << 1)) * 16));
  }
  const unsigned char* xb = (const unsigned char*)x + (size_t)g * 64 * 1280;
  const unsigned char* wb = (const unsigned char*)w1 + j * 384;
  auto stage = [&](int kc) {
    const unsigned sl = (unsigned)((kc & 3) * SLOT);
#pragma unroll
    for (int i = 0; i < 2; ++i) glds16(voffX[i], xb + kc * 128, sl + w * 2048 + i * 1024);
#pragma unroll
    for (int i = 0; i < 6; ++i) glds16(voffW[i], wb + (size_t)kc * 64 * 3072, sl + XPART + w * 6144 + i * 1024);
  };
  stage(0); stage(1); stage(2); stage(3);

  unsigned aX[2], aW[6];
#pragma unroll
  for (int s = 0; s < 2; ++s) aX[s] = (unsigned)((32 * wt + t) * 128 + (((4 * s + q) ^ (t >> 1)) * 16));
  const int gl = (qq >> 1) | ((q & 1) << 1);
#pragma unroll
  for (int i = 0; i < 6; ++i) aW[i] = (unsigned)(XPART + (8 * q + qq) * 384 + (((6 * wf + (i ^ wt)) ^ gl) * 32) + p * 8);
  auto rdfrags = [&](Frags& fr, int kc, int s) {
    const unsigned sl = (unsigned)((kc & 3) * SLOT);
#pragma unroll
    for (int T = 0; T < 2; ++T) fr.X[T] = lds_rd128(L, sl + aX[s] + ((T ^ wf) * 2048));
#pragma unroll
    for (int i = 0; i < 6; ++i) {
      const v2u lo = lds_tr(L, sl + aW[i] + s * 32 * 384), hi = lds_tr(L, sl + aW[i] + s * 32 * 384 + 4 * 384);
      fr.W[i] = v4u{lo.x, lo.y, hi.x, hi.y};
    }
  };
  f32x4 acc[6][2], cs[3], sx, gr;
#pragma unroll
  for (int i = 0; i < 6; ++i) { acc[i][0] = f32x4{0, 0, 0, 0}; acc[i][1] = f32x4{0, 0, 0, 0}; }
  cs[0] = cs[1] = cs[2] = sx = gr = f32x4{0, 0, 0, 0};
  const v4u ones = {0x3F803F80u, 0x3F803F80u, 0x3F803F80u, 0x3F803F80u};
  auto domfma = [&](const Frags& fr) {
#pragma unroll
    for (int i = 0; i < 6; ++i) {
      acc[i][0] = mfma(fr.W[i], fr.X[0], acc[i][0]);
      acc[i][1] = mfma(fr.W[i], fr.X[1], acc[i][1]);
      if ((i & 1) == 0) cs[i >> 1] = mfma(fr.W[i], ones, cs[i >> 1]);
    }
    sx = mfma(ones, fr.X[0], sx);
    gr = mfma(fr.X[0], fr.X[0], gr);
  };
  Frags F0, F1;
  wait_v<24>(); BAR(); SCHED();
  rdfrags(F0, 0, 0);
  {
    unsigned char* dst = ws + W2F_OFF;
#pragma unroll
    for (int cc = 0; cc < 2; ++cc) {
      const int c = 2 * w + cc;
      const unsigned a = (unsigned)(RT_OFF + (4 * q + qq) * 256 + c * 32 + p * 8);
      const v2u lo = lds_tr(L, a), hi = lds_tr(L, a + 16 * 256);
      const v4u v = {lo.x, lo.y, hi.x, hi.y};
      const unsigned off = (unsigned)(((rng * 8 + c) * 48 + rks) * 1024 + l * 16);
      asm volatile("global_store_dwordx4 %0, %1, %2\n\ts_nop 1" ::"v"(off), "v"(v), "s"(dst) : "memory");
    }
  }
  SCHED();
#define ITER(kc, NW_)                                              \
  {                                                                \
    rdfrags(F1, kc, 1);                                            \
    domfma(F0);                                                    \
    SCHED();                                                       \
    if (kc < 9) { WAIT_L0(); wait_v<NW_>(); BAR(); SCHED(); }      \
    if (kc + 4 <= 9) stage(kc + 4);                                \
    if (kc < 9) rdfrags(F0, kc + 1, 0);                            \
    domfma(F1);                                                    \
    SCHED();                                                       \
  }
  ITER(0, 18) ITER(1, 18) ITER(2, 18) ITER(3, 16) ITER(4, 16) ITER(5, 16) ITER(6, 16) ITER(7, 8) ITER(8, 0) ITER(9, 0)
#undef ITER
  if (t == 0) {
#pragma unroll
    for (int u = 0; u < 3; ++u) *(LAS f32x4*)(L + CS_OFF + (((wf * 2 + wt) * 3 + u) * 4 + q) * 16) = cs[u];
  }
  {
    const int r = t & 3;
    const float s2 = r == 0 ? gr[0] : r == 1 ? gr[1] : r == 2 ? gr[2] : gr[3];
    const float mean = sx[0] * (1.0f / D_);
    const float var = fmaxf(s2 * (1.0f / D_) - mean * mean, 0.0f);
    const float rstd = __builtin_amdgcn_rsqf(var + 1e-5f);
    if (q == qq) *(LAS f32x2*)(L + ST_OFF + ((wt * 2 + wf) * 16 + t) * 8) = f32x2{rstd, -mean * rstd};
  }
  WAIT_L0(); BAR(); SCHED();
  f32x4 csx[6];
#pragma unroll
  for (int u = 0; u < 3; ++u) { csx[2 * u] = cs[u]; csx[2 * u + 1] = *(LAS f32x4*)(L + CS_OFF + (((wf * 2 + (1 - wt)) * 3 + u) * 4 + q) * 16); }
  f32x2 st[2];
#pragma unroll
  for (int T = 0; T < 2; ++T) st[T] = *(LAS f32x2*)(L + ST_OFF + ((wt * 2 + (T ^ wf)) * 16 + t) * 8);
#pragma unroll
  for (int i = 0; i < 6; ++i) asm volatile("" : "+v"(b1r[i]));
  unsigned char* hb = ws + H_OFF;
#pragma unroll
  for (int T = 0; T < 2; ++T) {
#pragma unroll
    for (int u = 0; u < 3; ++u) {
      unsigned pk[2][2];
#pragma unroll
      for (int h2 = 0; h2 < 2; ++h2) {
        const int i = 2 * u + h2;
        const float bb[4] = {bf_lo(b1r[i].x), bf_hi(b1r[i].x), bf_lo(b1r[i].y), bf_hi(b1r[i].y)};
        float o[4];
#pragma unroll
        for (int r = 0; r < 4; ++r) o[r] = gelu_tanh(__builtin_fmaf(st[T].x, acc[i][T][r], __builtin_fmaf(st[T].y, csx[i][r], bb[r])));
        pk[h2][0] = pk_bf16(o[0], o[1]); pk[h2][1] = pk_bf16(o[2], o[3]);
      }
      const v4u v = wt ? v4u{pk[1][0], pk[1][1], pk[0][0], pk[0][1]} : v4u{pk[0][0], pk[0][1], pk[1][0], pk[1][1]};
      const unsigned off = (unsigned)((((g * 48 + 6 * j + 3 * wf + u) * 4) + 2 * wt + (T ^ wf)) * 1024 + l * 16);
      *(GAS v4u*)((GAS unsigned char*)hb + off) = v;
    }
  }
}

__global__ void __launch_bounds__(256, 1) k_phase2(const unsigned short* __restrict__ b2, const unsigned char* __restrict__ ws, unsigned short* __restrict__ out) {
  extern __shared__ __attribute__((aligned(16))) unsigned char smem[];
  LAS unsigned char* L = (LAS unsigned char*)smem;
  const int tid = threadIdx.x, l = tid & 63, w = __builtin_amdgcn_readfirstlane(tid >> 6);
  const int q = l >> 4, t = l & 15;
  const int b = blockIdx.x, xcd = b & 7, idx = b >> 3;
  const int g = xcd * 4 + (idx >> 3), j = idx & 7;
  const GAS v4u* wf = (const GAS v4u*)(ws + W2F_OFF) + (size_t)(5 * j * 48 + 12 * w) * 64 + l;
  const GAS v4u* hf = (const GAS v4u*)(ws + H_OFF) + (size_t)((g * 48 + 12 * w) * 4) * 64 + l;
  f32x4 acc[5][4];
#pragma unroll
  for (int i = 0; i < 5; ++i)
#pragma unroll
    for (int T = 0; T < 4; ++T) acc[i][T] = f32x4{0, 0, 0, 0};
  v4u A[12][5], B[12][4];
#pragma unroll
  for (int s = 0; s < 12; ++s) {
#pragma unroll
    for (int T = 0; T < 4; ++T) B[s][T] = hf[(s * 4 + T) * 64];
#pragma unroll
    for (int i = 0; i < 5; ++i) A[s][i] = wf[(i * 48 + s) * 64];
    if (s == 5) SCHED();
  }
#pragma unroll
  for (int s = 0; s < 12; ++s) {
#pragma unroll
    for (int i = 0; i < 5; ++i)
#pragma unroll
      for (int T = 0; T < 4; ++T) acc[i][T] = mfma(A[s][i], B[s][T], acc[i][T]);
  }
#pragma unroll
  for (int T = 0; T < 4; ++T)
#pragma unroll
    for (int i = 0; i < 5; ++i) *(LAS f32x4*)(L + (((w * 4 + T) * 5 + i) * 64 + l) * 16) = acc[i][T];
  __syncthreads();
#pragma unroll
  for (int i = 0; i < 5; ++i) {
    f32x4 s = *(LAS f32x4*)(L + (((0 * 4 + w) * 5 + i) * 64 + l) * 16);
#pragma unroll
    for (int ww = 1; ww < 4; ++ww) s += *(LAS f32x4*)(L + (((ww * 4 + w) * 5 + i) * 64 + l) * 16);
    const int n = 80 * j + 16 * i + 4 * q;
    const v2u bb = *(const GAS v2u*)(b2 + n);
    v2u o;
    o.x = pk_bf16(s[0] + bf_lo(bb.x), s[1] + bf_hi(bb.x));
    o.y = pk_bf16(s[2] + bf_lo(bb.y), s[3] + bf_hi(bb.y));
    *(GAS v2u*)(out + (size_t)(64 * g + 16 * w + t) * D_ + n) = o;
  }
}

extern "C" void kernel_launch(void* const* d_in, const int* in_sizes, int n_in, void* d_out, int out_size, void* d_ws, size_t ws_size, hipStream_t stream) {
  const unsigned short* x = (const unsigned short*)d_in[0];
  const unsigned short* w1 = (const unsigned short*)d_in[1];
  const unsigned short* b1 = (const unsigned short*)d_in[2];
  const unsigned short* w2 = (const unsigned short*)d_in[3];
  const unsigned short* b2 = (const unsigned short*)d_in[4];
  hipFuncSetAttribute((const void*)k_phase1, hipFuncAttributeMaxDynamicSharedMemorySize, LDS1_BYTES);
  hipFuncSetAttribute((const void*)k_phase2, hipFuncAttributeMaxDynamicSharedMemorySize, LDS2_BYTES);
  k_phase1<<<256, 256, LDS1_BYTES, stream>>>(x, w1, b1, w2, (unsigned char*)d_ws);
  k_phase2<<<256, 256, LDS2_BYTES, stream>>>(b2, (const unsigned char*)d_ws, (unsigned short*)d_out);
}
```

```cpp
#include <hip/hip_runtime.h>
#include <stdint.h>
__global__ void __launch_bounds__(256, 1) mixer_mlp_kernel(const void* x, const void* w1, const void* b1, const void* w2, const void* b2, void* out, void* ws) {
  asm volatile(
    "s_getpc_b64 s[90:91]\n"
    "Lcpc_2%=:\n"
    "v_lshlrev_b32 v2, 7, v0\n"
    "v_min_u32 v2, Lcend_1%=-Lcpc_2%=-4, v2\n"
    "global_load_dword v231, v2, s[90:91]\n"
    "s_mov_b64 s[8:9], %0\n"
    "s_mov_b32 s10, %1\n"
    "s_load_dwordx2 s[12:13], s[8:9], 0\n"
    "s_load_dwordx2 s[16:17], s[8:9], 8\n"
    "s_load_dwordx2 s[36:37], s[8:9], 48\n"
    "s_load_dwordx2 s[24:25], s[8:9], 24\n"
    "s_load_dwordx2 s[20:21], s[8:9], 16\n"
    "s_load_dwordx2 s[28:29], s[8:9], 32\n"
    "s_load_dwordx2 s[32:33], s[8:9], 40\n"
    "v_lshrrev_b32 v1, 6, v0\n"
    "v_and_b32 v240, 63, v0\n"
    "v_readfirstlane_b32 s40, v1\n"
    "s_mov_b32 s14, 2621440\n"
    "s_mov_b32 s15, 0x20000\n"
    "s_mov_b32 s18, 1966080\n"
    "s_mov_b32 s19, 0x20000\n"
    "s_mov_b32 s38, 6537216\n"
    "s_mov_b32 s39, 0x20000\n"
    "s_mov_b32 s26, 1966080\n"
    "s_mov_b32 s27, 0x20000\n"
    "s_mov_b32 s22, 3072\n"
    "s_mov_b32 s23, 0x20000\n"
    "s_mov_b32 s30, 1280\n"
    "s_mov_b32 s31, 0x20000\n"
    "s_mov_b32 s34, 2621440\n"
    "s_mov_b32 s35, 0x20000\n"
    "s_and_b32 s56, s10, 7\n"
    "s_lshr_b32 s57, s10, 3\n"
    "s_lshr_b32 s58, s56, 1\n"
    "s_lshl_b32 s43, s58, 3\n"
    "s_lshr_b32 s58, s57, 2\n"
    "s_add_u32 s43, s43, s58\n"
    "s_and_b32 s58, s56, 1\n"
    "s_lshl_b32 s44, s58, 2\n"
    "s_and_b32 s58, s57, 3\n"
    "s_add_u32 s44, s44, s58\n"
    "v_lshrrev_b32 v1, 3, v240\n"
    "v_and_b32 v2, 7, v240\n"
    "v_lshrrev_b32 v3, 4, v240\n"
    "v_xor_b32 v5, v2, v3\n"
    "v_mul_u32_u24 v6, 1280, v1\n"
    "v_lshl_add_u32 v12, v5, 4, v6\n"
    "v_and_b32 v5, 2, v1\n"
    "v_xor_b32 v5, v2, v5\n"
    "v_mul_u32_u24 v6, 3072, v1\n"
    "v_lshl_add_u32 v14, v5, 4, v6\n"
    "v_xor_b32 v13, 64, v12\n"
    "v_xor_b32 v15, 64, v14\n"
    "v_add_u32 v13, 9216, v13\n"
    "v_add_u32 v15, 23552, v15\n"
    "s_lshl_b32 s47, s40, 11\n"
    "s_lshl_b32 s58, s43, 2\n"
    "s_add_u32 s58, s58, s40\n"
    "s_mul_i32 s48, s58, 20480\n"
    "s_mul_i32 s49, s44, 384\n"
    "s_mul_i32 s58, s40, 49152\n"
    "s_add_u32 s49, s49, s58\n"
    "s_getpc_b64 s[94:95]\n"
    "Lpc_4%=:\n"
    "s_add_u32 s94, s94, Ltab_3%=-Lpc_4%=\n"
    "s_addc_u32 s95, s95, 0\n"
    "s_and_b32 s58, s43, 3\n"
    "s_lshl_b32 s58, s58, 2\n"
    "s_and_b32 s59, s44, 3\n"
    "s_add_u32 s58, s58, s59\n"
    "s_lshl_b32 s58, s58, 4\n"
    "s_load_dwordx2 s[66:67], s[94:95], s58\n"
    "s_and_b32 s56, s10, 7\n"
    "s_lshr_b32 s57, s10, 3\n"
    "s_lshr_b32 s58, s57, 3\n"
    "s_lshl_b32 s45, s56, 2\n"
    "s_add_u32 s45, s45, s58\n"
    "s_and_b32 s46, s57, 7\n"
    "s_and_b32 s58, s45, 3\n"
    "s_lshl_b32 s58, s58, 2\n"
    "s_and_b32 s59, s46, 3\n"
    "s_add_u32 s58, s58, s59\n"
    "s_lshl_b32 s58, s58, 4\n"
    "s_add_u32 s58, s58, 8\n"
    "s_load_dwordx2 s[92:93], s[94:95], s58\n"
    "s_mov_b32 s50, s48\n"
    "s_mov_b32 s51, s49\n"
    "s_waitcnt lgkmcnt(0)\n"
    "s_lshr_b64 s[8:9], s[66:67], 0\n"
    "s_and_b32 s8, s8, 15\n"
    "s_lshl_b32 s9, s8, 7\n"
    "s_add_u32 s48, s50, s9\n"
    "s_mul_i32 s9, s8, 196608\n"
    "s_add_u32 s49, s51, s9\n"
    "s_add_u32 m0, s47, 0\n"
    "s_nop 0\n"
    "buffer_load_dwordx4 v12, s[12:15], s48 offen offset:0 lds\n"
    "buffer_load_dwordx4 v13, s[12:15], s48 offen offset:1024 lds\n"
    "s_add_u32 m0, s47, 8192\n"
    "s_nop 0\n"
    "buffer_load_dwordx4 v14, s[16:19], s49 offen offset:0 lds\n"
    "buffer_load_dwordx4 v15, s[16:19], s49 offen offset:1024 lds\n"
    "s_add_u32 m0, s47, 16256\n"
    "s_nop 0\n"
    "buffer_load_dwordx4 v14, s[16:19], s49 offen offset:128 lds\n"
    "buffer_load_dwordx4 v15, s[16:19], s49 offen offset:1152 lds\n"
    "s_add_u32 m0, s47, 24320\n"
    "s_nop 0\n"
    "buffer_load_dwordx4 v14, s[16:19], s49 offen offset:256 lds\n"
    "buffer_load_dwordx4 v15, s[16:19], s49 offen offset:1280 lds\n"
    "s_lshr_b64 s[8:9], s[66:67], 4\n"
    "s_and_b32 s8, s8, 15\n"
    "s_lshl_b32 s9, s8, 7\n"
    "s_add_u32 s48, s50, s9\n"
    "s_mul_i32 s9, s8, 196608\n"
    "s_add_u32 s49, s51, s9\n"
    "s_add_u32 m0, s47, 32768\n"
    "s_nop 0\n"
    "buffer_load_dwordx4 v12, s[12:15], s48 offen offset:0 lds\n"
    "buffer_load_dwordx4 v13, s[12:15], s48 offen offset:1024 lds\n"
    "s_add_u32 m0, s47, 40960\n"
    "s_nop 0\n"
    "buffer_load_dwordx4 v14, s[16:19], s49 offen offset:0 lds\n"
    "buffer_load_dwordx4 v15, s[16:19], s49 offen offset:1024 lds\n"
    "s_add_u32 m0, s47, 49024\n"
    "s_nop 0\n"
    "buffer_load_dwordx4 v14, s[16:19], s49 offen offset:128 lds\n"
    "buffer_load_dwordx4 v15, s[16:19], s49 offen offset:1152 lds\n"
    "s_add_u32 m0, s47, 57088\n"
    "s_nop 0\n"
    "buffer_load_dwordx4 v14, s[16:19], s49 offen offset:256 lds\n"
    "buffer_load_dwordx4 v15, s[16:19], s49 offen offset:1280 lds\n"
    "v_mov_b32 v10, 0x7ffffff0\n"
    "s_cmp_eq_u32 s40, 0\n"
    "s_cbranch_scc0 Lu1_5%=\n"
    "v_lshlrev_b32 v9, 7, v240\n"
    "v_cmp_gt_u32 vcc, 36, v240\n"
    "s_nop 1\n"
    "v_cndmask_b32 v9, v10, v9, vcc\n"
    "s_mul_i32 s58, s10, 4608\n"
    "s_add_u32 s58, s58, 786432\n"
    "buffer_load_dword v230, v9, s[16:19], s58 offen\n"
    "s_branch Lu3_7%=\n"
    "Lu1_5%=:\n"
    "s_cmp_eq_u32 s40, 1\n"
    "s_cbranch_scc0 Lu2_6%=\n"
    "v_mul_u32_u24 v9, 10923, v240\n"
    "v_lshrrev_b32 v9, 16, v9\n"
    "v_mul_u32_u24 v11, 6, v9\n"
    "v_sub_u32 v11, v240, v11\n"
    "v_mul_u32_u24 v9, 1280, v9\n"
    "v_lshl_add_u32 v9, v11, 7, v9\n"
    "v_cmp_gt_u32 vcc, 48, v240\n"
    "s_nop 1\n"
    "v_cndmask_b32 v9, v10, v9, vcc\n"
    "s_lshl_b32 s58, s43, 3\n"
    "s_add_u32 s58, s58, s44\n"
    "s_mul_i32 s58, s58, 10240\n"
    "s_add_u32 s58, s58, 512\n"
    "buffer_load_dword v230, v9, s[12:15], s58 offen\n"
    "s_branch Lu3_7%=\n"
    "Lu2_6%=:\n"
    "s_cmp_eq_u32 s40, 3\n"
    "s_cbranch_scc0 Lu3_7%=\n"
    "v_lshlrev_b32 v9, 7, v240\n"
    "v_cmp_gt_u32 vcc, 60, v240\n"
    "s_nop 1\n"
    "v_cndmask_b32 v9, v10, v9, vcc\n"
    "s_mul_i32 s58, s10, 7680\n"
    "buffer_load_dword v230, v9, s[24:27], s58 offen\n"
    "Lu3_7%=:\n"
    "s_and_b32 s41, s40, 1\n"
    "s_lshr_b32 s42, s40, 1\n"
    "v_and_b32 v4, 15, v240\n"
    "buffer_load_dwordx2 v[232:233], off, s[36:39], 0 sc1\n"
    "s_lshl_b32 s58, s42, 5\n"
    "v_lshl_add_u32 v234, v3, 3, s58\n"
    "s_sub_u32 s58, 32, s58\n"
    "v_lshl_add_u32 v235, v3, 3, s58\n"
    "s_mul_i32 s58, s44, 384\n"
    "s_mul_i32 s59, s41, 192\n"
    "s_add_u32 s58, s58, s59\n"
    "buffer_load_dwordx2 v[172:173], v234, s[20:23], s58 offen offset:0\n"
    "buffer_load_dwordx2 v[174:175], v235, s[20:23], s58 offen offset:0\n"
    "buffer_load_dwordx2 v[176:177], v234, s[20:23], s58 offen offset:64\n"
    "buffer_load_dwordx2 v[178:179], v235, s[20:23], s58 offen offset:64\n"
    "buffer_load_dwordx2 v[180:181], v234, s[20:23], s58 offen offset:128\n"
    "buffer_load_dwordx2 v[182:183], v235, s[20:23], s58 offen offset:128\n"
    "v_lshrrev_b32 v6, 2, v4\n"
    "v_and_b32 v7, 3, v240\n"
    "v_lshrrev_b32 v9, 1, v4\n"
    "v_xor_b32 v9, v3, v9\n"
    "v_lshlrev_b32 v9, 4, v9\n"
    "v_lshl_add_u32 v9, v4, 7, v9\n"
    "s_lshl_b32 s58, s42, 12\n"
    "s_lshl_b32 s59, s41, 11\n"
    "s_add_u32 s60, s58, s59\n"
    "v_add_u32 v16, s60, v9\n"
    "s_xor_b32 s59, s59, 2048\n"
    "s_add_u32 s60, s58, s59\n"
    "v_add_u32 v18, s60, v9\n"
    "v_xor_b32 v17, 64, v16\n"
    "v_xor_b32 v19, 64, v18\n"
    "v_lshrrev_b32 v8, 1, v6\n"
    "v_and_b32 v9, 1, v3\n"
    "v_lshl_or_b32 v8, v9, 1, v8\n"
    "v_lshl_add_u32 v9, v3, 3, v6\n"
    "v_lshlrev_b32 v9, 7, v9\n"
    "v_lshl_add_u32 v9, v7, 3, v9\n"
    "s_mul_i32 s58, s41, 6\n"
    "s_xor_b32 s59, s42, 0\n"
    "s_add_u32 s59, s59, s58\n"
    "s_and_b32 s60, s59, 3\n"
    "s_lshr_b32 s59, s59, 2\n"
    "s_lshl_b32 s59, s59, 13\n"
    "s_add_u32 s59, s59, 8192\n"
    "v_xor_b32 v10, s60, v8\n"
    "v_lshl_add_u32 v10, v10, 5, v9\n"
    "v_add_u32 v24, s59, v10\n"
    "s_xor_b32 s59, s42, 1\n"
    "s_add_u32 s59, s59, s58\n"
    "s_and_b32 s60, s59, 3\n"
    "s_lshr_b32 s59, s59, 2\n"
    "s_lshl_b32 s59, s59, 13\n"
    "s_add_u32 s59, s59, 8192\n"
    "v_xor_b32 v10, s60, v8\n"
    "v_lshl_add_u32 v10, v10, 5, v9\n"
    "v_add_u32 v25, s59, v10\n"
    "s_xor_b32 s59, s42, 2\n"
    "s_add_u32 s59, s59, s58\n"
    "s_and_b32 s60, s59, 3\n"
    "s_lshr_b32 s59, s59, 2\n"
    "s_lshl_b32 s59, s59, 13\n"
    "s_add_u32 s59, s59, 8192\n"
    "v_xor_b32 v10, s60, v8\n"
    "v_lshl_add_u32 v10, v10, 5, v9\n"
    "v_add_u32 v26, s59, v10\n"
    "s_xor_b32 s59, s42, 3\n"
    "s_add_u32 s59, s59, s58\n"
    "s_and_b32 s60, s59, 3\n"
    "s_lshr_b32 s59, s59, 2\n"
    "s_lshl_b32 s59, s59, 13\n"
    "s_add_u32 s59, s59, 8192\n"
    "v_xor_b32 v10, s60, v8\n"
    "v_lshl_add_u32 v10, v10, 5, v9\n"
    "v_add_u32 v27, s59, v10\n"
    "s_xor_b32 s59, s42, 4\n"
    "s_add_u32 s59, s59, s58\n"
    "s_and_b32 s60, s59, 3\n"
    "s_lshr_b32 s59, s59, 2\n"
    "s_lshl_b32 s59, s59, 13\n"
    "s_add_u32 s59, s59, 8192\n"
    "v_xor_b32 v10, s60, v8\n"
    "v_lshl_add_u32 v10, v10, 5, v9\n"
    "v_add_u32 v28, s59, v10\n"
    "s_xor_b32 s59, s42, 5\n"
    "s_add_u32 s59, s59, s58\n"
    "s_and_b32 s60, s59, 3\n"
    "s_lshr_b32 s59, s59, 2\n"
    "s_lshl_b32 s59, s59, 13\n"
    "s_add_u32 s59, s59, 8192\n"
    "v_xor_b32 v10, s60, v8\n"
    "v_lshl_add_u32 v10, v10, 5, v9\n"
    "v_add_u32 v29, s59, v10\n"
    "v_mov_b32 v168, 0x3f803f80\n"
    "v_mov_b32 v169, 0x3f803f80\n"
    "v_mov_b32 v170, 0x3f803f80\n"
    "v_mov_b32 v171, 0x3f803f80\n"
    "v_add_u32 v20, 0x10000, v16\n"
    "v_add_u32 v21, 0x10000, v17\n"
    "v_add_u32 v22, 0x10000, v18\n"
    "v_add_u32 v23, 0x10000, v19\n"
    "v_add_u32 v30, 0x10000, v24\n"
    "v_add_u32 v31, 0x10000, v25\n"
    "v_add_u32 v32, 0x10000, v26\n"
    "v_add_u32 v33, 0x10000, v27\n"
    "v_add_u32 v34, 0x10000, v28\n"
    "v_add_u32 v35, 0x10000, v29\n"
    "s_lshl_b32 s58, s41, 1\n"
    "s_add_u32 s59, s58, s42\n"
    "s_mul_i32 s59, s59, 192\n"
    "s_add_u32 s59, s59, 136192\n"
    "v_lshl_add_u32 v224, v3, 4, s59\n"
    "s_xor_b32 s59, s42, 1\n"
    "s_add_u32 s59, s58, s59\n"
    "s_mul_i32 s59, s59, 192\n"
    "s_add_u32 s59, s59, 136192\n"
    "v_lshl_add_u32 v225, v3, 4, s59\n"
    "s_lshl_b32 s58, s42, 1\n"
    "s_add_u32 s59, s58, s41\n"
    "s_lshl_b32 s59, s59, 7\n"
    "s_add_u32 s59, s59, 137216\n"
    "v_lshl_add_u32 v226, v4, 3, s59\n"
    "v_xor_b32 v227, 128, v226\n"
    "s_mov_b32 s54, 0xbdd2d3e8\n"
    "s_mov_b32 s55, 0xc0135761\n"
    "s_cmp_eq_u32 s42, 1\n"
    "s_cselect_b64 s[62:63], -1, 0\n"
    "s_mul_i32 s58, s43, 48\n"
    "s_mul_i32 s59, s44, 6\n"
    "s_add_u32 s58, s58, s59\n"
    "s_mul_i32 s59, s41, 3\n"
    "s_add_u32 s58, s58, s59\n"
    "s_lshl_b32 s58, s58, 2\n"
    "s_lshl_b32 s59, s42, 1\n"
    "s_add_u32 s58, s58, s59\n"
    "s_add_u32 s59, s58, s41\n"
    "s_lshl_b32 s59, s59, 10\n"
    "s_add_u32 s64, s59, 65536\n"
    "s_xor_b32 s59, s41, 1\n"
    "s_add_u32 s59, s58, s59\n"
    "s_lshl_b32 s59, s59, 10\n"
    "s_add_u32 s65, s59, 65536\n"
    "s_and_b32 s56, s10, 7\n"
    "s_lshr_b32 s57, s10, 3\n"
    "s_lshr_b32 s58, s57, 3\n"
    "s_lshl_b32 s45, s56, 2\n"
    "s_add_u32 s45, s45, s58\n"
    "s_and_b32 s46, s57, 7\n"
    "s_mul_i32 s58, s40, 491520\n"
    "s_mul_i32 s59, s46, 160\n"
    "s_add_u32 s11, s58, s59\n"
    "s_mov_b32 s4, s11\n"
    "s_lshl_b32 s58, s45, 8\n"
    "s_lshl_b32 s59, s40, 6\n"
    "s_add_u32 s58, s58, s59\n"
    "s_add_u32 s99, s58, 8192\n"
    "s_mul_i32 s58, s45, 48\n"
    "s_mul_i32 s59, s40, 12\n"
    "s_add_u32 s58, s58, s59\n"
    "s_lshl_b32 s58, s58, 12\n"
    "s_add_u32 s97, s58, 65536\n"
    "s_mov_b32 s3, s97\n"
    "v_add_u32 v9, 0, v240\n"
    "v_mul_u32_u24 v10, 205, v9\n"
    "v_lshrrev_b32 v10, 11, v10\n"
    "v_mul_u32_u24 v11, 10, v10\n"
    "v_sub_u32 v9, v9, v11\n"
    "v_mul_u32_u24 v10, 1280, v10\n"
    "v_lshl_add_u32 v251, v9, 4, v10\n"
    "v_add_u32 v9, 64, v240\n"
    "v_mul_u32_u24 v10, 205, v9\n"
    "v_lshrrev_b32 v10, 11, v10\n"
    "v_mul_u32_u24 v11, 10, v10\n"
    "v_sub_u32 v9, v9, v11\n"
    "v_mul_u32_u24 v10, 1280, v10\n"
    "v_lshl_add_u32 v252, v9, 4, v10\n"
    "v_subrev_u32 v252, 1024, v252\n"
    "v_add_u32 v9, 128, v240\n"
    "v_mul_u32_u24 v10, 205, v9\n"
    "v_lshrrev_b32 v10, 11, v10\n"
    "v_mul_u32_u24 v11, 10, v10\n"
    "v_sub_u32 v9, v9, v11\n"
    "v_mul_u32_u24 v10, 1280, v10\n"
    "v_lshl_add_u32 v253, v9, 4, v10\n"
    "v_subrev_u32 v253, 2048, v253\n"
    "v_add_u32 v9, 192, v240\n"
    "v_mul_u32_u24 v10, 205, v9\n"
    "v_lshrrev_b32 v10, 11, v10\n"
    "v_mul_u32_u24 v11, 10, v10\n"
    "v_sub_u32 v9, v9, v11\n"
    "v_mul_u32_u24 v10, 1280, v10\n"
    "v_lshl_add_u32 v254, v9, 4, v10\n"
    "v_subrev_u32 v254, 3072, v254\n"
    "v_add_u32 v9, 256, v240\n"
    "v_mul_u32_u24 v10, 205, v9\n"
    "v_lshrrev_b32 v10, 11, v10\n"
    "v_mul_u32_u24 v11, 10, v10\n"
    "v_sub_u32 v9, v9, v11\n"
    "v_mul_u32_u24 v10, 1280, v10\n"
    "v_lshl_add_u32 v255, v9, 4, v10\n"
    "v_lshl_add_u32 v9, v3, 2, v6\n"
    "v_mul_u32_u24 v9, 160, v9\n"
    "v_lshl_add_u32 v9, v7, 3, v9\n"
    "s_lshl_b32 s7, s40, 13\n"
    "v_add_u32 v248, s7, v9\n"
    "v_add_u32 v249, 0x10000, v248\n"
    "v_add_u32 v250, 0x20000, v248\n"
    "s_waitcnt vmcnt(15)\n"
    "s_barrier\n"
    "ds_read_b128 v[36:39], v16 offset:0\n"
    "ds_read_b64_tr_b16 v[44:45], v24 offset:0\n"
    "ds_read_b64_tr_b16 v[46:47], v24 offset:512\n"
    "ds_read_b128 v[40:43], v18 offset:0\n"
    "ds_read_b64_tr_b16 v[48:49], v25 offset:0\n"
    "ds_read_b64_tr_b16 v[50:51], v25 offset:512\n"
    "ds_read_b64_tr_b16 v[52:53], v26 offset:0\n"
    "ds_read_b64_tr_b16 v[54:55], v26 offset:512\n"
    "ds_read_b64_tr_b16 v[56:57], v27 offset:0\n"
    "ds_read_b64_tr_b16 v[58:59], v27 offset:512\n"
    "ds_read_b64_tr_b16 v[60:61], v28 offset:0\n"
    "ds_read_b64_tr_b16 v[62:63], v28 offset:512\n"
    "ds_read_b64_tr_b16 v[64:65], v29 offset:0\n"
    "ds_read_b64_tr_b16 v[66:67], v29 offset:512\n"
    "s_waitcnt lgkmcnt(10)\n"
    "v_mfma_f32_16x16x32_bf16 v[100:103], v[44:47], v[36:39], 0\n"
    "ds_read_b128 v[68:71], v17 offset:0\n"
    "ds_read_b64_tr_b16 v[76:77], v24 offset:4096\n"
    "v_mfma_f32_16x16x32_bf16 v[104:107], v[44:47], v[40:43], 0\n"
    "ds_read_b64_tr_b16 v[78:79], v24 offset:4608\n"
    "ds_read_b128 v[72:75], v19 offset:0\n"
    "v_mfma_f32_16x16x32_bf16 v[148:151], v[44:47], v[168:171], 0\n"
    "ds_read_b64_tr_b16 v[80:81], v25 offset:4096\n"
    "s_lshr_b64 s[8:9], s[66:67], 8\n"
    "s_waitcnt lgkmcnt(5)\n"
    "v_mfma_f32_16x16x32_bf16 v[108:111], v[48:51], v[36:39], 0\n"
    "s_and_b32 s8, s8, 15\n"
    "v_mfma_f32_16x16x32_bf16 v[112:115], v[48:51], v[40:43], 0\n"
    "s_lshl_b32 s9, s8, 7\n"
    "s_add_u32 s48, s50, s9\n"
    "v_mfma_f32_16x16x32_bf16 v[116:119], v[52:55], v[36:39], 0\n"
    "s_mul_i32 s9, s8, 196608\n"
    "s_add_u32 s49, s51, s9\n"
    "v_mfma_f32_16x16x32_bf16 v[120:123], v[52:55], v[40:43], 0\n"
    "s_add_u32 m0, s47, 65536\n"
    "v_mfma_f32_16x16x32_bf16 v[152:155], v[52:55], v[168:171], 0\n"
    "buffer_load_dwordx4 v12, s[12:15], s48 offen offset:0 lds\n"
    "buffer_load_dwordx4 v13, s[12:15], s48 offen offset:1024 lds\n"
    "v_mfma_f32_16x16x32_bf16 v[124:127], v[56:59], v[36:39], 0\n"
    "ds_read_b64_tr_b16 v[82:83], v25 offset:4608\n"
    "ds_read_b64_tr_b16 v[84:85], v26 offset:4096\n"
    "v_mfma_f32_16x16x32_bf16 v[128:131], v[56:59], v[40:43], 0\n"
    "ds_read_b64_tr_b16 v[86:87], v26 offset:4608\n"
    "ds_read_b64_tr_b16 v[88:89], v27 offset:4096\n"
    "v_mfma_f32_16x16x32_bf16 v[132:135], v[60:63], v[36:39], 0\n"
    "ds_read_b64_tr_b16 v[90:91], v27 offset:4608\n"
    "v_mfma_f32_16x16x32_bf16 v[136:139], v[60:63], v[40:43], 0\n"
    "s_add_u32 m0, s47, 73728\n"
    "v_mfma_f32_16x16x32_bf16 v[156:159], v[60:63], v[168:171], 0\n"
    "buffer_load_dwordx4 v14, s[16:19], s49 offen offset:0 lds\n"
    "buffer_load_dwordx4 v15, s[16:19], s49 offen offset:1024 lds\n"
    "ds_read_b64_tr_b16 v[92:93], v28 offset:4096\n"
    "v_mfma_f32_16x16x32_bf16 v[140:143], v[64:67], v[36:39], 0\n"
    "ds_read_b64_tr_b16 v[94:95], v28 offset:4608\n"
    "v_mfma_f32_16x16x32_bf16 v[144:147], v[64:67], v[40:43], 0\n"
    "ds_read_b64_tr_b16 v[96:97], v29 offset:4096\n"
    "ds_read_b64_tr_b16 v[98:99], v29 offset:4608\n"
    "v_mfma_f32_16x16x32_bf16 v[160:163], v[168:171], v[36:39], 0\n"
    "s_add_u32 m0, s47, 81792\n"
    "v_mfma_f32_16x16x32_bf16 v[164:167], v[36:39], v[36:39], 0\n"
    "buffer_load_dwordx4 v14, s[16:19], s49 offen offset:128 lds\n"
    "buffer_load_dwordx4 v15, s[16:19], s49 offen offset:1152 lds\n"
    "s_waitcnt lgkmcnt(0)\n"
    "s_waitcnt vmcnt(13)\n"
    "s_barrier\n"
    "v_mfma_f32_16x16x32_bf16 v[100:103], v[76:79], v[68:71], v[100:103]\n"
    "ds_read_b128 v[36:39], v16 offset:32768\n"
    "ds_read_b64_tr_b16 v[44:45], v24 offset:32768\n"
    "v_mfma_f32_16x16x32_bf16 v[104:107], v[76:79], v[72:75], v[104:107]\n"
    "ds_read_b64_tr_b16 v[46:47], v24 offset:33280\n"
    "ds_read_b128 v[40:43], v18 offset:32768\n"
    "v_mfma_f32_16x16x32_bf16 v[148:151], v[76:79], v[168:171], v[148:151]\n"
    "ds_read_b64_tr_b16 v[48:49], v25 offset:32768\n"
    "s_add_u32 m0, s47, 89856\n"
    "v_mfma_f32_16x16x32_bf16 v[108:111], v[80:83], v[68:71], v[108:111]\n"
    "buffer_load_dwordx4 v14, s[16:19], s49 offen offset:256 lds\n"
    "v_mfma_f32_16x16x32_bf16 v[112:115], v[80:83], v[72:75], v[112:115]\n"
    "buffer_load_dwordx4 v15, s[16:19], s49 offen offset:1280 lds\n"
    "ds_read_b64_tr_b16 v[50:51], v25 offset:33280\n"
    "v_mfma_f32_16x16x32_bf16 v[116:119], v[84:87], v[68:71], v[116:119]\n"
    "ds_read_b64_tr_b16 v[52:53], v26 offset:32768\n"
    "ds_read_b64_tr_b16 v[54:55], v26 offset:33280\n"
    "v_mfma_f32_16x16x32_bf16 v[120:123], v[84:87], v[72:75], v[120:123]\n"
    "ds_read_b64_tr_b16 v[56:57], v27 offset:32768\n"
    "v_mfma_f32_16x16x32_bf16 v[152:155], v[84:87], v[168:171], v[152:155]\n"
    "ds_read_b64_tr_b16 v[58:59], v27 offset:33280\n"
    "s_lshr_b64 s[8:9], s[66:67], 12\n"
    "v_mfma_f32_16x16x32_bf16 v[124:127], v[88:91], v[68:71], v[124:127]\n"
    "s_and_b32 s8, s8, 15\n"
    "s_lshl_b32 s9, s8, 7\n"
    "v_mfma_f32_16x16x32_bf16 v[128:131], v[88:91], v[72:75], v[128:131]\n"
    "s_add_u32 s48, s50, s9\n"
    "s_mul_i32 s9, s8, 196608\n"
    "v_mfma_f32_16x16x32_bf16 v[132:135], v[92:95], v[68:71], v[132:135]\n"
    "s_add_u32 s49, s51, s9\n"
    "v_mfma_f32_16x16x32_bf16 v[136:139], v[92:95], v[72:75], v[136:139]\n"
    "s_add_u32 m0, s47, 98304\n"
    "v_mfma_f32_16x16x32_bf16 v[156:159], v[92:95], v[168:171], v[156:159]\n"
    "buffer_load_dwordx4 v12, s[12:15], s48 offen offset:0 lds\n"
    "buffer_load_dwordx4 v13, s[12:15], s48 offen offset:1024 lds\n"
    "ds_read_b64_tr_b16 v[60:61], v28 offset:32768\n"
    "v_mfma_f32_16x16x32_bf16 v[140:143], v[96:99], v[68:71], v[140:143]\n"
    "ds_read_b64_tr_b16 v[62:63], v28 offset:33280\n"
    "v_mfma_f32_16x16x32_bf16 v[144:147], v[96:99], v[72:75], v[144:147]\n"
    "ds_read_b64_tr_b16 v[64:65], v29 offset:32768\n"
    "ds_read_b64_tr_b16 v[66:67], v29 offset:33280\n"
    "v_mfma_f32_16x16x32_bf16 v[160:163], v[168:171], v[68:71], v[160:163]\n"
    "s_add_u32 m0, s47, 106496\n"
    "v_mfma_f32_16x16x32_bf16 v[164:167], v[68:71], v[68:71], v[164:167]\n"
    "buffer_load_dwordx4 v14, s[16:19], s49 offen offset:0 lds\n"
    "buffer_load_dwordx4 v15, s[16:19], s49 offen offset:1024 lds\n"
    "s_waitcnt lgkmcnt(10)\n"
    "v_mfma_f32_16x16x32_bf16 v[100:103], v[44:47], v[36:39], v[100:103]\n"
    "ds_read_b128 v[68:71], v17 offset:32768\n"
    "ds_read_b64_tr_b16 v[76:77], v24 offset:36864\n"
    "v_mfma_f32_16x16x32_bf16 v[104:107], v[44:47], v[40:43], v[104:107]\n"
    "ds_read_b64_tr_b16 v[78:79], v24 offset:37376\n"
    "ds_read_b128 v[72:75], v19 offset:32768\n"
    "v_mfma_f32_16x16x32_bf16 v[148:151], v[44:47], v[168:171], v[148:151]\n"
    "ds_read_b64_tr_b16 v[80:81], v25 offset:36864\n"
    "s_add_u32 m0, s47, 114560\n"
    "s_waitcnt lgkmcnt(5)\n"
    "v_mfma_f32_16x16x32_bf16 v[108:111], v[48:51], v[36:39], v[108:111]\n"
    "buffer_load_dwordx4 v14, s[16:19], s49 offen offset:128 lds\n"
    "v_mfma_f32_16x16x32_bf16 v[112:115], v[48:51], v[40:43], v[112:115]\n"
    "buffer_load_dwordx4 v15, s[16:19], s49 offen offset:1152 lds\n"
    "ds_read_b64_tr_b16 v[82:83], v25 offset:37376\n"
    "v_mfma_f32_16x16x32_bf16 v[116:119], v[52:55], v[36:39], v[116:119]\n"
    "ds_read_b64_tr_b16 v[84:85], v26 offset:36864\n"
    "ds_read_b64_tr_b16 v[86:87], v26 offset:37376\n"
    "v_mfma_f32_16x16x32_bf16 v[120:123], v[52:55], v[40:43], v[120:123]\n"
    "ds_read_b64_tr_b16 v[88:89], v27 offset:36864\n"
    "v_mfma_f32_16x16x32_bf16 v[152:155], v[52:55], v[168:171], v[152:155]\n"
    "ds_read_b64_tr_b16 v[90:91], v27 offset:37376\n"
    "s_add_u32 m0, s47, 122624\n"
    "v_mfma_f32_16x16x32_bf16 v[124:127], v[56:59], v[36:39], v[124:127]\n"
    "buffer_load_dwordx4 v14, s[16:19], s49 offen offset:256 lds\n"
    "buffer_load_dwordx4 v15, s[16:19], s49 offen offset:1280 lds\n"
    "v_mfma_f32_16x16x32_bf16 v[128:131], v[56:59], v[40:43], v[128:131]\n"
    "ds_read_b64_tr_b16 v[92:93], v28 offset:36864\n"
    "ds_read_b64_tr_b16 v[94:95], v28 offset:37376\n"
    "v_mfma_f32_16x16x32_bf16 v[132:135], v[60:63], v[36:39], v[132:135]\n"
    "ds_read_b64_tr_b16 v[96:97], v29 offset:36864\n"
    "v_mfma_f32_16x16x32_bf16 v[136:139], v[60:63], v[40:43], v[136:139]\n"
    "ds_read_b64_tr_b16 v[98:99], v29 offset:37376\n"
    "s_lshr_b64 s[8:9], s[66:67], 16\n"
    "v_mfma_f32_16x16x32_bf16 v[156:159], v[60:63], v[168:171], v[156:159]\n"
    "s_and_b32 s8, s8, 15\n"
    "s_lshl_b32 s9, s8, 7\n"
    "v_mfma_f32_16x16x32_bf16 v[140:143], v[64:67], v[36:39], v[140:143]\n"
    "s_add_u32 s48, s50, s9\n"
    "v_mfma_f32_16x16x32_bf16 v[144:147], v[64:67], v[40:43], v[144:147]\n"
    "s_mul_i32 s9, s8, 196608\n"
    "s_add_u32 s49, s51, s9\n"
    "v_mfma_f32_16x16x32_bf16 v[160:163], v[168:171], v[36:39], v[160:163]\n"
    "s_add_u32 m0, s47, 0\n"
    "v_mfma_f32_16x16x32_bf16 v[164:167], v[36:39], v[36:39], v[164:167]\n"
    "buffer_load_dwordx4 v12, s[12:15], s48 offen offset:0 lds\n"
    "buffer_load_dwordx4 v13, s[12:15], s48 offen offset:1024 lds\n"
    "s_waitcnt lgkmcnt(0)\n"
    "s_waitcnt vmcnt(10)\n"
    "s_barrier\n"
    "v_mfma_f32_16x16x32_bf16 v[100:103], v[76:79], v[68:71], v[100:103]\n"
    "ds_read_b128 v[36:39], v20 offset:0\n"
    "ds_read_b64_tr_b16 v[44:45], v30 offset:0\n"
    "v_mfma_f32_16x16x32_bf16 v[104:107], v[76:79], v[72:75], v[104:107]\n"
    "ds_read_b64_tr_b16 v[46:47], v30 offset:512\n"
    "v_mfma_f32_16x16x32_bf16 v[148:151], v[76:79], v[168:171], v[148:151]\n"
    "ds_read_b128 v[40:43], v22 offset:0\n"
    "ds_read_b64_tr_b16 v[48:49], v31 offset:0\n"
    "v_mfma_f32_16x16x32_bf16 v[108:111], v[80:83], v[68:71], v[108:111]\n"
    "s_add_u32 m0, s47, 8192\n"
    "v_mfma_f32_16x16x32_bf16 v[112:115], v[80:83], v[72:75], v[112:115]\n"
    "buffer_load_dwordx4 v14, s[16:19], s49 offen offset:0 lds\n"
    "v_mfma_f32_16x16x32_bf16 v[116:119], v[84:87], v[68:71], v[116:119]\n"
    "buffer_load_dwordx4 v15, s[16:19], s49 offen offset:1024 lds\n"
    "ds_read_b64_tr_b16 v[50:51], v31 offset:512\n"
    "v_mfma_f32_16x16x32_bf16 v[120:123], v[84:87], v[72:75], v[120:123]\n"
    "ds_read_b64_tr_b16 v[52:53], v32 offset:0\n"
    "v_mfma_f32_16x16x32_bf16 v[152:155], v[84:87], v[168:171], v[152:155]\n"
    "ds_read_b64_tr_b16 v[54:55], v32 offset:512\n"
    "v_mfma_f32_16x16x32_bf16 v[124:127], v[88:91], v[68:71], v[124:127]\n"
    "ds_read_b64_tr_b16 v[56:57], v33 offset:0\n"
    "ds_read_b64_tr_b16 v[58:59], v33 offset:512\n"
    "v_mfma_f32_16x16x32_bf16 v[128:131], v[88:91], v[72:75], v[128:131]\n"
    "s_add_u32 m0, s47, 16256\n"
    "v_mfma_f32_16x16x32_bf16 v[132:135], v[92:95], v[68:71], v[132:135]\n"
    "buffer_load_dwordx4 v14, s[16:19], s49 offen offset:128 lds\n"
    "v_mfma_f32_16x16x32_bf16 v[136:139], v[92:95], v[72:75], v[136:139]\n"
    "buffer_load_dwordx4 v15, s[16:19], s49 offen offset:1152 lds\n"
    "ds_read_b64_tr_b16 v[60:61], v34 offset:0\n"
    "v_mfma_f32_16x16x32_bf16 v[156:159], v[92:95], v[168:171], v[156:159]\n"
    "ds_read_b64_tr_b16 v[62:63], v34 offset:512\n"
    "v_mfma_f32_16x16x32_bf16 v[140:143], v[96:99], v[68:71], v[140:143]\n"
    "ds_read_b64_tr_b16 v[64:65], v35 offset:0\n"
    "v_mfma_f32_16x16x32_bf16 v[144:147], v[96:99], v[72:75], v[144:147]\n"
    "ds_read_b64_tr_b16 v[66:67], v35 offset:512\n"
    "s_add_u32 m0, s47, 24320\n"
    "v_mfma_f32_16x16x32_bf16 v[160:163], v[168:171], v[68:71], v[160:163]\n"
    "buffer_load_dwordx4 v14, s[16:19], s49 offen offset:256 lds\n"
    "v_mfma_f32_16x16x32_bf16 v[164:167], v[68:71], v[68:71], v[164:167]\n"
    "buffer_load_dwordx4 v15, s[16:19], s49 offen offset:1280 lds\n"
    "s_waitcnt lgkmcnt(10)\n"
    "v_mfma_f32_16x16x32_bf16 v[100:103], v[44:47], v[36:39], v[100:103]\n"
    "ds_read_b128 v[68:71], v21 offset:0\n"
    "ds_read_b64_tr_b16 v[76:77], v30 offset:4096\n"
    "v_mfma_f32_16x16x32_bf16 v[104:107], v[44:47], v[40:43], v[104:107]\n"
    "ds_read_b64_tr_b16 v[78:79], v30 offset:4608\n"
    "ds_read_b128 v[72:75], v23 offset:0\n"
    "v_mfma_f32_16x16x32_bf16 v[148:151], v[44:47], v[168:171], v[148:151]\n"
    "ds_read_b64_tr_b16 v[80:81], v31 offset:4096\n"
    "s_waitcnt lgkmcnt(5)\n"
    "v_mfma_f32_16x16x32_bf16 v[108:111], v[48:51], v[36:39], v[108:111]\n"
    "ds_read_b64_tr_b16 v[82:83], v31 offset:4608\n"
    "ds_read_b64_tr_b16 v[84:85], v32 offset:4096\n"
    "v_mfma_f32_16x16x32_bf16 v[112:115], v[48:51], v[40:43], v[112:115]\n"
    "s_lshr_b64 s[8:9], s[66:67], 20\n"
    "v_mfma_f32_16x16x32_bf16 v[116:119], v[52:55], v[36:39], v[116:119]\n"
    "s_and_b32 s8, s8, 15\n"
    "s_lshl_b32 s9, s8, 7\n"
    "v_mfma_f32_16x16x32_bf16 v[120:123], v[52:55], v[40:43], v[120:123]\n"
    "s_add_u32 s48, s50, s9\n"
    "v_mfma_f32_16x16x32_bf16 v[152:155], v[52:55], v[168:171], v[152:155]\n"
    "s_mul_i32 s9, s8, 196608\n"
    "s_add_u32 s49, s51, s9\n"
    "v_mfma_f32_16x16x32_bf16 v[124:127], v[56:59], v[36:39], v[124:127]\n"
    "s_add_u32 m0, s47, 32768\n"
    "v_mfma_f32_16x16x32_bf16 v[128:131], v[56:59], v[40:43], v[128:131]\n"
    "buffer_load_dwordx4 v12, s[12:15], s48 offen offset:0 lds\n"
    "buffer_load_dwordx4 v13, s[12:15], s48 offen offset:1024 lds\n"
    "v_mfma_f32_16x16x32_bf16 v[132:135], v[60:63], v[36:39], v[132:135]\n"
    "ds_read_b64_tr_b16 v[86:87], v32 offset:4608\n"
    "v_mfma_f32_16x16x32_bf16 v[136:139], v[60:63], v[40:43], v[136:139]\n"
    "ds_read_b64_tr_b16 v[88:89], v33 offset:4096\n"
    "ds_read_b64_tr_b16 v[90:91], v33 offset:4608\n"
    "v_mfma_f32_16x16x32_bf16 v[156:159], v[60:63], v[168:171], v[156:159]\n"
    "ds_read_b64_tr_b16 v[92:93], v34 offset:4096\n"
    "v_mfma_f32_16x16x32_bf16 v[140:143], v[64:67], v[36:39], v[140:143]\n"
    "ds_read_b64_tr_b16 v[94:95], v34 offset:4608\n"
    "ds_read_b64_tr_b16 v[96:97], v35 offset:4096\n"
    "v_mfma_f32_16x16x32_bf16 v[144:147], v[64:67], v[40:43], v[144:147]\n"
    "ds_read_b64_tr_b16 v[98:99], v35 offset:4608\n"
    "v_mfma_f32_16x16x32_bf16 v[160:163], v[168:171], v[36:39], v[160:163]\n"
    "s_add_u32 m0, s47, 40960\n"
    "v_mfma_f32_16x16x32_bf16 v[164:167], v[36:39], v[36:39], v[164:167]\n"
    "buffer_load_dwordx4 v14, s[16:19], s49 offen offset:0 lds\n"
    "buffer_load_dwordx4 v15, s[16:19], s49 offen offset:1024 lds\n"
    "s_waitcnt lgkmcnt(0)\n"
    "s_waitcnt vmcnt(12)\n"
    "s_barrier\n"
    "v_mfma_f32_16x16x32_bf16 v[100:103], v[76:79], v[68:71], v[100:103]\n"
    "ds_read_b128 v[36:39], v20 offset:32768\n"
    "ds_read_b64_tr_b16 v[44:45], v30 offset:32768\n"
    "v_mfma_f32_16x16x32_bf16 v[104:107], v[76:79], v[72:75], v[104:107]\n"
    "ds_read_b64_tr_b16 v[46:47], v30 offset:33280\n"
    "v_mfma_f32_16x16x32_bf16 v[148:151], v[76:79], v[168:171], v[148:151]\n"
    "ds_read_b128 v[40:43], v22 offset:32768\n"
    "v_mfma_f32_16x16x32_bf16 v[108:111], v[80:83], v[68:71], v[108:111]\n"
    "ds_read_b64_tr_b16 v[48:49], v31 offset:32768\n"
    "v_mfma_f32_16x16x32_bf16 v[112:115], v[80:83], v[72:75], v[112:115]\n"
    "ds_read_b64_tr_b16 v[50:51], v31 offset:33280\n"
    "v_mfma_f32_16x16x32_bf16 v[116:119], v[84:87], v[68:71], v[116:119]\n"
    "ds_read_b64_tr_b16 v[52:53], v32 offset:32768\n"
    "s_add_u32 m0, s47, 49024\n"
    "v_mfma_f32_16x16x32_bf16 v[120:123], v[84:87], v[72:75], v[120:123]\n"
    "buffer_load_dwordx4 v14, s[16:19], s49 offen offset:128 lds\n"
    "v_mfma_f32_16x16x32_bf16 v[152:155], v[84:87], v[168:171], v[152:155]\n"
    "buffer_load_dwordx4 v15, s[16:19], s49 offen offset:1152 lds\n"
    "v_mfma_f32_16x16x32_bf16 v[124:127], v[88:91], v[68:71], v[124:127]\n"
    "ds_read_b64_tr_b16 v[54:55], v32 offset:33280\n"
    "v_mfma_f32_16x16x32_bf16 v[128:131], v[88:91], v[72:75], v[128:131]\n"
    "ds_read_b64_tr_b16 v[56:57], v33 offset:32768\n"
    "v_mfma_f32_16x16x32_bf16 v[132:135], v[92:95], v[68:71], v[132:135]\n"
    "ds_read_b64_tr_b16 v[58:59], v33 offset:33280\n"
    "v_mfma_f32_16x16x32_bf16 v[136:139], v[92:95], v[72:75], v[136:139]\n"
    "ds_read_b64_tr_b16 v[60:61], v34 offset:32768\n"
    "ds_read_b64_tr_b16 v[62:63], v34 offset:33280\n"
    "v_mfma_f32_16x16x32_bf16 v[156:159], v[92:95], v[168:171], v[156:159]\n"
    "ds_read_b64_tr_b16 v[64:65], v35 offset:32768\n"
    "v_mfma_f32_16x16x32_bf16 v[140:143], v[96:99], v[68:71], v[140:143]\n"
    "ds_read_b64_tr_b16 v[66:67], v35 offset:33280\n"
    "v_mfma_f32_16x16x32_bf16 v[144:147], v[96:99], v[72:75], v[144:147]\n"
    "s_add_u32 m0, s47, 57088\n"
    "v_mfma_f32_16x16x32_bf16 v[160:163], v[168:171], v[68:71], v[160:163]\n"
    "buffer_load_dwordx4 v14, s[16:19], s49 offen offset:256 lds\n"
    "v_mfma_f32_16x16x32_bf16 v[164:167], v[68:71], v[68:71], v[164:167]\n"
    "buffer_load_dwordx4 v15, s[16:19], s49 offen offset:1280 lds\n"
    "s_waitcnt lgkmcnt(10)\n"
    "v_mfma_f32_16x16x32_bf16 v[100:103], v[44:47], v[36:39], v[100:103]\n"
    "ds_read_b128 v[68:71], v21 offset:32768\n"
    "ds_read_b64_tr_b16 v[76:77], v30 offset:36864\n"
    "v_mfma_f32_16x16x32_bf16 v[104:107], v[44:47], v[40:43], v[104:107]\n"
    "ds_read_b64_tr_b16 v[78:79], v30 offset:37376\n"
    "ds_read_b128 v[72:75], v23 offset:32768\n"
    "v_mfma_f32_16x16x32_bf16 v[148:151], v[44:47], v[168:171], v[148:151]\n"
    "ds_read_b64_tr_b16 v[80:81], v31 offset:36864\n"
    "s_waitcnt lgkmcnt(5)\n"
    "v_mfma_f32_16x16x32_bf16 v[108:111], v[48:51], v[36:39], v[108:111]\n"
    "ds_read_b64_tr_b16 v[82:83], v31 offset:37376\n"
    "ds_read_b64_tr_b16 v[84:85], v32 offset:36864\n"
    "v_mfma_f32_16x16x32_bf16 v[112:115], v[48:51], v[40:43], v[112:115]\n"
    "s_lshr_b64 s[8:9], s[66:67], 24\n"
    "v_mfma_f32_16x16x32_bf16 v[116:119], v[52:55], v[36:39], v[116:119]\n"
    "s_and_b32 s8, s8, 15\n"
    "s_lshl_b32 s9, s8, 7\n"
    "v_mfma_f32_16x16x32_bf16 v[120:123], v[52:55], v[40:43], v[120:123]\n"
    "s_add_u32 s48, s50, s9\n"
    "v_mfma_f32_16x16x32_bf16 v[152:155], v[52:55], v[168:171], v[152:155]\n"
    "s_mul_i32 s9, s8, 196608\n"
    "s_add_u32 s49, s51, s9\n"
    "v_mfma_f32_16x16x32_bf16 v[124:127], v[56:59], v[36:39], v[124:127]\n"
    "s_add_u32 m0, s47, 65536\n"
    "v_mfma_f32_16x16x32_bf16 v[128:131], v[56:59], v[40:43], v[128:131]\n"
    "buffer_load_dwordx4 v12, s[12:15], s48 offen offset:0 lds\n"
    "buffer_load_dwordx4 v13, s[12:15], s48 offen offset:1024 lds\n"
    "v_mfma_f32_16x16x32_bf16 v[132:135], v[60:63], v[36:39], v[132:135]\n"
    "ds_read_b64_tr_b16 v[86:87], v32 offset:37376\n"
    "v_mfma_f32_16x16x32_bf16 v[136:139], v[60:63], v[40:43], v[136:139]\n"
    "ds_read_b64_tr_b16 v[88:89], v33 offset:36864\n"
    "ds_read_b64_tr_b16 v[90:91], v33 offset:37376\n"
    "v_mfma_f32_16x16x32_bf16 v[156:159], v[60:63], v[168:171], v[156:159]\n"
    "ds_read_b64_tr_b16 v[92:93], v34 offset:36864\n"
    "v_mfma_f32_16x16x32_bf16 v[140:143], v[64:67], v[36:39], v[140:143]\n"
    "ds_read_b64_tr_b16 v[94:95], v34 offset:37376\n"
    "ds_read_b64_tr_b16 v[96:97], v35 offset:36864\n"
    "v_mfma_f32_16x16x32_bf16 v[144:147], v[64:67], v[40:43], v[144:147]\n"
    "ds_read_b64_tr_b16 v[98:99], v35 offset:37376\n"
    "v_mfma_f32_16x16x32_bf16 v[160:163], v[168:171], v[36:39], v[160:163]\n"
    "s_add_u32 m0, s47, 73728\n"
    "v_mfma_f32_16x16x32_bf16 v[164:167], v[36:39], v[36:39], v[164:167]\n"
    "buffer_load_dwordx4 v14, s[16:19], s49 offen offset:0 lds\n"
    "buffer_load_dwordx4 v15, s[16:19], s49 offen offset:1024 lds\n"
    "s_waitcnt lgkmcnt(0)\n"
    "s_waitcnt vmcnt(12)\n"
    "s_barrier\n"
    "v_mfma_f32_16x16x32_bf16 v[100:103], v[76:79], v[68:71], v[100:103]\n"
    "ds_read_b128 v[36:39], v16 offset:0\n"
    "ds_read_b64_tr_b16 v[44:45], v24 offset:0\n"
    "v_mfma_f32_16x16x32_bf16 v[104:107], v[76:79], v[72:75], v[104:107]\n"
    "ds_read_b64_tr_b16 v[46:47], v24 offset:512\n"
    "v_mfma_f32_16x16x32_bf16 v[148:151], v[76:79], v[168:171], v[148:151]\n"
    "s_add_u32 m0, s47, 81792\n"
    "v_mfma_f32_16x16x32_bf16 v[108:111], v[80:83], v[68:71], v[108:111]\n"
    "buffer_load_dwordx4 v14, s[16:19], s49 offen offset:128 lds\n"
    "buffer_load_dwordx4 v15, s[16:19], s49 offen offset:1152 lds\n"
    "v_mfma_f32_16x16x32_bf16 v[112:115], v[80:83], v[72:75], v[112:115]\n"
    "ds_read_b128 v[40:43], v18 offset:0\n"
    "ds_read_b64_tr_b16 v[48:49], v25 offset:0\n"
    "v_mfma_f32_16x16x32_bf16 v[116:119], v[84:87], v[68:71], v[116:119]\n"
    "ds_read_b64_tr_b16 v[50:51], v25 offset:512\n"
    "v_mfma_f32_16x16x32_bf16 v[120:123], v[84:87], v[72:75], v[120:123]\n"
    "s_add_u32 m0, s47, 89856\n"
    "v_mfma_f32_16x16x32_bf16 v[152:155], v[84:87], v[168:171], v[152:155]\n"
    "buffer_load_dwordx4 v14, s[16:19], s49 offen offset:256 lds\n"
    "buffer_load_dwordx4 v15, s[16:19], s49 offen offset:1280 lds\n"
    "v_mfma_f32_16x16x32_bf16 v[124:127], v[88:91], v[68:71], v[124:127]\n"
    "ds_read_b64_tr_b16 v[52:53], v26 offset:0\n"
    "ds_read_b64_tr_b16 v[54:55], v26 offset:512\n"
    "v_mfma_f32_16x16x32_bf16 v[128:131], v[88:91], v[72:75], v[128:131]\n"
    "ds_read_b64_tr_b16 v[56:57], v27 offset:0\n"
    "v_mfma_f32_16x16x32_bf16 v[132:135], v[92:95], v[68:71], v[132:135]\n"
    "v_readfirstlane_b32 s52, v232\n"
    "v_mfma_f32_16x16x32_bf16 v[136:139], v[92:95], v[72:75], v[136:139]\n"
    "ds_read_b64_tr_b16 v[58:59], v27 offset:512\n"
    "v_mfma_f32_16x16x32_bf16 v[156:159], v[92:95], v[168:171], v[156:159]\n"
    "ds_read_b64_tr_b16 v[60:61], v28 offset:0\n"
    "ds_read_b64_tr_b16 v[62:63], v28 offset:512\n"
    "v_mfma_f32_16x16x32_bf16 v[140:143], v[96:99], v[68:71], v[140:143]\n"
    "v_readfirstlane_b32 s53, v233\n"
    "v_mfma_f32_16x16x32_bf16 v[144:147], v[96:99], v[72:75], v[144:147]\n"
    "ds_read_b64_tr_b16 v[64:65], v29 offset:0\n"
    "ds_read_b64_tr_b16 v[66:67], v29 offset:512\n"
    "v_mfma_f32_16x16x32_bf16 v[160:163], v[168:171], v[68:71], v[160:163]\n"
    "s_add_u32 s52, s52, 0x7f4a7c15\n"
    "v_mfma_f32_16x16x32_bf16 v[164:167], v[68:71], v[68:71], v[164:167]\n"
    "s_addc_u32 s53, s53, 0x9e3779b9\n"
    "s_waitcnt lgkmcnt(10)\n"
    "v_mfma_f32_16x16x32_bf16 v[100:103], v[44:47], v[36:39], v[100:103]\n"
    "ds_read_b128 v[68:71], v17 offset:0\n"
    "ds_read_b64_tr_b16 v[76:77], v24 offset:4096\n"
    "v_mfma_f32_16x16x32_bf16 v[104:107], v[44:47], v[40:43], v[104:107]\n"
    "ds_read_b64_tr_b16 v[78:79], v24 offset:4608\n"
    "ds_read_b128 v[72:75], v19 offset:0\n"
    "v_mfma_f32_16x16x32_bf16 v[148:151], v[44:47], v[168:171], v[148:151]\n"
    "ds_read_b64_tr_b16 v[80:81], v25 offset:4096\n"
    "s_waitcnt lgkmcnt(5)\n"
    "v_mfma_f32_16x16x32_bf16 v[108:111], v[48:51], v[36:39], v[108:111]\n"
    "ds_read_b64_tr_b16 v[82:83], v25 offset:4608\n"
    "ds_read_b64_tr_b16 v[84:85], v26 offset:4096\n"
    "v_mfma_f32_16x16x32_bf16 v[112:115], v[48:51], v[40:43], v[112:115]\n"
    "s_lshr_b64 s[8:9], s[66:67], 28\n"
    "v_mfma_f32_16x16x32_bf16 v[116:119], v[52:55], v[36:39], v[116:119]\n"
    "s_and_b32 s8, s8, 15\n"
    "s_lshl_b32 s9, s8, 7\n"
    "v_mfma_f32_16x16x32_bf16 v[120:123], v[52:55], v[40:43], v[120:123]\n"
    "s_add_u32 s48, s50, s9\n"
    "v_mfma_f32_16x16x32_bf16 v[152:155], v[52:55], v[168:171], v[152:155]\n"
    "s_mul_i32 s9, s8, 196608\n"
    "s_add_u32 s49, s51, s9\n"
    "v_mfma_f32_16x16x32_bf16 v[124:127], v[56:59], v[36:39], v[124:127]\n"
    "s_add_u32 m0, s47, 98304\n"
    "v_mfma_f32_16x16x32_bf16 v[128:131], v[56:59], v[40:43], v[128:131]\n"
    "buffer_load_dwordx4 v12, s[12:15], s48 offen offset:0 lds\n"
    "buffer_load_dwordx4 v13, s[12:15], s48 offen offset:1024 lds\n"
    "v_mfma_f32_16x16x32_bf16 v[132:135], v[60:63], v[36:39], v[132:135]\n"
    "ds_read_b64_tr_b16 v[86:87], v26 offset:4608\n"
    "v_mfma_f32_16x16x32_bf16 v[136:139], v[60:63], v[40:43], v[136:139]\n"
    "ds_read_b64_tr_b16 v[88:89], v27 offset:4096\n"
    "ds_read_b64_tr_b16 v[90:91], v27 offset:4608\n"
    "v_mfma_f32_16x16x32_bf16 v[156:159], v[60:63], v[168:171], v[156:159]\n"
    "ds_read_b64_tr_b16 v[92:93], v28 offset:4096\n"
    "v_mfma_f32_16x16x32_bf16 v[140:143], v[64:67], v[36:39], v[140:143]\n"
    "ds_read_b64_tr_b16 v[94:95], v28 offset:4608\n"
    "ds_read_b64_tr_b16 v[96:97], v29 offset:4096\n"
    "v_mfma_f32_16x16x32_bf16 v[144:147], v[64:67], v[40:43], v[144:147]\n"
    "ds_read_b64_tr_b16 v[98:99], v29 offset:4608\n"
    "v_mfma_f32_16x16x32_bf16 v[160:163], v[168:171], v[36:39], v[160:163]\n"
    "s_add_u32 m0, s47, 106496\n"
    "v_mfma_f32_16x16x32_bf16 v[164:167], v[36:39], v[36:39], v[164:167]\n"
    "buffer_load_dwordx4 v14, s[16:19], s49 offen offset:0 lds\n"
    "buffer_load_dwordx4 v15, s[16:19], s49 offen offset:1024 lds\n"
    "s_waitcnt lgkmcnt(0)\n"
    "s_waitcnt vmcnt(12)\n"
    "s_barrier\n"
    "v_mfma_f32_16x16x32_bf16 v[100:103], v[76:79], v[68:71], v[100:103]\n"
    "ds_read_b128 v[36:39], v16 offset:32768\n"
    "ds_read_b64_tr_b16 v[44:45], v24 offset:32768\n"
    "v_mfma_f32_16x16x32_bf16 v[104:107], v[76:79], v[72:75], v[104:107]\n"
    "ds_read_b64_tr_b16 v[46:47], v24 offset:33280\n"
    "v_mfma_f32_16x16x32_bf16 v[148:151], v[76:79], v[168:171], v[148:151]\n"
    "ds_read_b128 v[40:43], v18 offset:32768\n"
    "v_mfma_f32_16x16x32_bf16 v[108:111], v[80:83], v[68:71], v[108:111]\n"
    "ds_read_b64_tr_b16 v[48:49], v25 offset:32768\n"
    "v_mfma_f32_16x16x32_bf16 v[112:115], v[80:83], v[72:75], v[112:115]\n"
    "ds_read_b64_tr_b16 v[50:51], v25 offset:33280\n"
    "v_mfma_f32_16x16x32_bf16 v[116:119], v[84:87], v[68:71], v[116:119]\n"
    "ds_read_b64_tr_b16 v[52:53], v26 offset:32768\n"
    "s_add_u32 m0, s47, 114560\n"
    "v_mfma_f32_16x16x32_bf16 v[120:123], v[84:87], v[72:75], v[120:123]\n"
    "buffer_load_dwordx4 v14, s[16:19], s49 offen offset:128 lds\n"
    "v_mfma_f32_16x16x32_bf16 v[152:155], v[84:87], v[168:171], v[152:155]\n"
    "buffer_load_dwordx4 v15, s[16:19], s49 offen offset:1152 lds\n"
    "v_mfma_f32_16x16x32_bf16 v[124:127], v[88:91], v[68:71], v[124:127]\n"
    "ds_read_b64_tr_b16 v[54:55], v26 offset:33280\n"
    "v_mfma_f32_16x16x32_bf16 v[128:131], v[88:91], v[72:75], v[128:131]\n"
    "ds_read_b64_tr_b16 v[56:57], v27 offset:32768\n"
    "v_mfma_f32_16x16x32_bf16 v[132:135], v[92:95], v[68:71], v[132:135]\n"
    "ds_read_b64_tr_b16 v[58:59], v27 offset:33280\n"
    "v_mfma_f32_16x16x32_bf16 v[136:139], v[92:95], v[72:75], v[136:139]\n"
    "ds_read_b64_tr_b16 v[60:61], v28 offset:32768\n"
    "ds_read_b64_tr_b16 v[62:63], v28 offset:33280\n"
    "v_mfma_f32_16x16x32_bf16 v[156:159], v[92:95], v[168:171], v[156:159]\n"
    "ds_read_b64_tr_b16 v[64:65], v29 offset:32768\n"
    "v_mfma_f32_16x16x32_bf16 v[140:143], v[96:99], v[68:71], v[140:143]\n"
    "ds_read_b64_tr_b16 v[66:67], v29 offset:33280\n"
    "v_mfma_f32_16x16x32_bf16 v[144:147], v[96:99], v[72:75], v[144:147]\n"
    "s_add_u32 m0, s47, 122624\n"
    "v_mfma_f32_16x16x32_bf16 v[160:163], v[168:171], v[68:71], v[160:163]\n"
    "buffer_load_dwordx4 v14, s[16:19], s49 offen offset:256 lds\n"
    "v_mfma_f32_16x16x32_bf16 v[164:167], v[68:71], v[68:71], v[164:167]\n"
    "buffer_load_dwordx4 v15, s[16:19], s49 offen offset:1280 lds\n"
    "s_waitcnt lgkmcnt(10)\n"
    "v_mfma_f32_16x16x32_bf16 v[100:103], v[44:47], v[36:39], v[100:103]\n"
    "ds_read_b128 v[68:71], v17 offset:32768\n"
    "ds_read_b64_tr_b16 v[76:77], v24 offset:36864\n"
    "v_mfma_f32_16x16x32_bf16 v[104:107], v[44:47], v[40:43], v[104:107]\n"
    "ds_read_b64_tr_b16 v[78:79], v24 offset:37376\n"
    "ds_read_b128 v[72:75], v19 offset:32768\n"
    "v_mfma_f32_16x16x32_bf16 v[148:151], v[44:47], v[168:171], v[148:151]\n"
    "ds_read_b64_tr_b16 v[80:81], v25 offset:36864\n"
    "s_waitcnt lgkmcnt(5)\n"
    "v_mfma_f32_16x16x32_bf16 v[108:111], v[48:51], v[36:39], v[108:111]\n"
    "ds_read_b64_tr_b16 v[82:83], v25 offset:37376\n"
    "ds_read_b64_tr_b16 v[84:85], v26 offset:36864\n"
    "v_mfma_f32_16x16x32_bf16 v[112:115], v[48:51], v[40:43], v[112:115]\n"
    "s_lshr_b64 s[8:9], s[66:67], 32\n"
    "v_mfma_f32_16x16x32_bf16 v[116:119], v[52:55], v[36:39], v[116:119]\n"
    "s_and_b32 s8, s8, 15\n"
    "s_lshl_b32 s9, s8, 7\n"
    "v_mfma_f32_16x16x32_bf16 v[120:123], v[52:55], v[40:43], v[120:123]\n"
    "s_add_u32 s48, s50, s9\n"
    "v_mfma_f32_16x16x32_bf16 v[152:155], v[52:55], v[168:171], v[152:155]\n"
    "s_mul_i32 s9, s8, 196608\n"
    "s_add_u32 s49, s51, s9\n"
    "v_mfma_f32_16x16x32_bf16 v[124:127], v[56:59], v[36:39], v[124:127]\n"
    "s_add_u32 m0, s47, 0\n"
    "v_mfma_f32_16x16x32_bf16 v[128:131], v[56:59], v[40:43], v[128:131]\n"
    "buffer_load_dwordx4 v12, s[12:15], s48 offen offset:0 lds\n"
    "buffer_load_dwordx4 v13, s[12:15], s48 offen offset:1024 lds\n"
    "v_mfma_f32_16x16x32_bf16 v[132:135], v[60:63], v[36:39], v[132:135]\n"
    "ds_read_b64_tr_b16 v[86:87], v26 offset:37376\n"
    "v_mfma_f32_16x16x32_bf16 v[136:139], v[60:63], v[40:43], v[136:139]\n"
    "ds_read_b64_tr_b16 v[88:89], v27 offset:36864\n"
    "ds_read_b64_tr_b16 v[90:91], v27 offset:37376\n"
    "v_mfma_f32_16x16x32_bf16 v[156:159], v[60:63], v[168:171], v[156:159]\n"
    "ds_read_b64_tr_b16 v[92:93], v28 offset:36864\n"
    "v_mfma_f32_16x16x32_bf16 v[140:143], v[64:67], v[36:39], v[140:143]\n"
    "ds_read_b64_tr_b16 v[94:95], v28 offset:37376\n"
    "ds_read_b64_tr_b16 v[96:97], v29 offset:36864\n"
    "v_mfma_f32_16x16x32_bf16 v[144:147], v[64:67], v[40:43], v[144:147]\n"
    "ds_read_b64_tr_b16 v[98:99], v29 offset:37376\n"
    "v_mfma_f32_16x16x32_bf16 v[160:163], v[168:171], v[36:39], v[160:163]\n"
    "s_add_u32 m0, s47, 8192\n"
    "v_mfma_f32_16x16x32_bf16 v[164:167], v[36:39], v[36:39], v[164:167]\n"
    "buffer_load_dwordx4 v14, s[16:19], s49 offen offset:0 lds\n"
    "buffer_load_dwordx4 v15, s[16:19], s49 offen offset:1024 lds\n"
    "s_waitcnt lgkmcnt(0)\n"
    "s_waitcnt vmcnt(12)\n"
    "s_barrier\n"
    "v_mfma_f32_16x16x32_bf16 v[100:103], v[76:79], v[68:71], v[100:103]\n"
    "ds_read_b128 v[36:39], v20 offset:0\n"
    "s_add_u32 m0, s47, 16256\n"
    "v_mfma_f32_16x16x32_bf16 v[104:107], v[76:79], v[72:75], v[104:107]\n"
    "buffer_load_dwordx4 v14, s[16:19], s49 offen offset:128 lds\n"
    "buffer_load_dwordx4 v15, s[16:19], s49 offen offset:1152 lds\n"
    "v_mfma_f32_16x16x32_bf16 v[148:151], v[76:79], v[168:171], v[148:151]\n"
    "ds_read_b64_tr_b16 v[44:45], v30 offset:0\n"
    "s_add_u32 m0, s47, 24320\n"
    "v_mfma_f32_16x16x32_bf16 v[108:111], v[80:83], v[68:71], v[108:111]\n"
    "buffer_load_dwordx4 v14, s[16:19], s49 offen offset:256 lds\n"
    "buffer_load_dwordx4 v15, s[16:19], s49 offen offset:1280 lds\n"
    "v_mfma_f32_16x16x32_bf16 v[112:115], v[80:83], v[72:75], v[112:115]\n"
    "ds_read_b64_tr_b16 v[46:47], v30 offset:512\n"
    "v_lshlrev_b32 v184, 16, v172\n"
    "v_mfma_f32_16x16x32_bf16 v[116:119], v[84:87], v[68:71], v[116:119]\n"
    "ds_read_b128 v[40:43], v22 offset:0\n"
    "v_and_b32 v185, 0xffff0000, v172\n"
    "v_mfma_f32_16x16x32_bf16 v[120:123], v[84:87], v[72:75], v[120:123]\n"
    "ds_read_b64_tr_b16 v[48:49], v31 offset:0\n"
    "v_lshlrev_b32 v186, 16, v173\n"
    "v_mfma_f32_16x16x32_bf16 v[152:155], v[84:87], v[168:171], v[152:155]\n"
    "ds_read_b64_tr_b16 v[50:51], v31 offset:512\n"
    "v_and_b32 v187, 0xffff0000, v173\n"
    "v_mfma_f32_16x16x32_bf16 v[124:127], v[88:91], v[68:71], v[124:127]\n"
    "ds_read_b64_tr_b16 v[52:53], v32 offset:0\n"
    "v_mfma_f32_16x16x32_bf16 v[128:131], v[88:91], v[72:75], v[128:131]\n"
    "v_lshlrev_b32 v188, 16, v174\n"
    "ds_read_b64_tr_b16 v[54:55], v32 offset:512\n"
    "v_mfma_f32_16x16x32_bf16 v[132:135], v[92:95], v[68:71], v[132:135]\n"
    "v_and_b32 v189, 0xffff0000, v174\n"
    "ds_read_b64_tr_b16 v[56:57], v33 offset:0\n"
    "v_mfma_f32_16x16x32_bf16 v[136:139], v[92:95], v[72:75], v[136:139]\n"
    "v_lshlrev_b32 v190, 16, v175\n"
    "ds_read_b64_tr_b16 v[58:59], v33 offset:512\n"
    "v_mfma_f32_16x16x32_bf16 v[156:159], v[92:95], v[168:171], v[156:159]\n"
    "v_and_b32 v191, 0xffff0000, v175\n"
    "ds_read_b64_tr_b16 v[60:61], v34 offset:0\n"
    "v_mfma_f32_16x16x32_bf16 v[140:143], v[96:99], v[68:71], v[140:143]\n"
    "v_lshlrev_b32 v192, 16, v176\n"
    "ds_read_b64_tr_b16 v[62:63], v34 offset:512\n"
    "v_mfma_f32_16x16x32_bf16 v[144:147], v[96:99], v[72:75], v[144:147]\n"
    "v_and_b32 v193, 0xffff0000, v176\n"
    "ds_read_b64_tr_b16 v[64:65], v35 offset:0\n"
    "v_mfma_f32_16x16x32_bf16 v[160:163], v[168:171], v[68:71], v[160:163]\n"
    "v_lshlrev_b32 v194, 16, v177\n"
    "ds_read_b64_tr_b16 v[66:67], v35 offset:512\n"
    "v_mfma_f32_16x16x32_bf16 v[164:167], v[68:71], v[68:71], v[164:167]\n"
    "v_and_b32 v195, 0xffff0000, v177\n"
    "s_waitcnt lgkmcnt(10)\n"
    "v_mfma_f32_16x16x32_bf16 v[100:103], v[44:47], v[36:39], v[100:103]\n"
    "ds_read_b128 v[68:71], v21 offset:0\n"
    "s_lshr_b64 s[8:9], s[66:67], 36\n"
    "s_and_b32 s8, s8, 15\n"
    "v_mfma_f32_16x16x32_bf16 v[104:107], v[44:47], v[40:43], v[104:107]\n"
    "s_lshl_b32 s9, s8, 7\n"
    "s_add_u32 s48, s50, s9\n"
    "v_mfma_f32_16x16x32_bf16 v[148:151], v[44:47], v[168:171], v[148:151]\n"
    "s_mul_i32 s9, s8, 196608\n"
    "s_add_u32 s49, s51, s9\n"
    "s_waitcnt lgkmcnt(1)\n"
    "v_mfma_f32_16x16x32_bf16 v[108:111], v[48:51], v[36:39], v[108:111]\n"
    "s_add_u32 m0, s47, 32768\n"
    "v_mfma_f32_16x16x32_bf16 v[112:115], v[48:51], v[40:43], v[112:115]\n"
    "buffer_load_dwordx4 v12, s[12:15], s48 offen offset:0 lds\n"
    "buffer_load_dwordx4 v13, s[12:15], s48 offen offset:1024 lds\n"
    "ds_read_b64_tr_b16 v[76:77], v30 offset:4096\n"
    "s_add_u32 m0, s47, 40960\n"
    "v_mfma_f32_16x16x32_bf16 v[116:119], v[52:55], v[36:39], v[116:119]\n"
    "buffer_load_dwordx4 v14, s[16:19], s49 offen offset:0 lds\n"
    "buffer_load_dwordx4 v15, s[16:19], s49 offen offset:1024 lds\n"
    "v_mfma_f32_16x16x32_bf16 v[120:123], v[52:55], v[40:43], v[120:123]\n"
    "ds_read_b64_tr_b16 v[78:79], v30 offset:4608\n"
    "v_lshlrev_b32 v196, 16, v178\n"
    "v_mfma_f32_16x16x32_bf16 v[152:155], v[52:55], v[168:171], v[152:155]\n"
    "ds_read_b128 v[72:75], v23 offset:0\n"
    "v_and_b32 v197, 0xffff0000, v178\n"
    "v_mfma_f32_16x16x32_bf16 v[124:127], v[56:59], v[36:39], v[124:127]\n"
    "ds_read_b64_tr_b16 v[80:81], v31 offset:4096\n"
    "v_lshlrev_b32 v198, 16, v179\n"
    "ds_read_b64_tr_b16 v[82:83], v31 offset:4608\n"
    "v_mfma_f32_16x16x32_bf16 v[128:131], v[56:59], v[40:43], v[128:131]\n"
    "v_and_b32 v199, 0xffff0000, v179\n"
    "ds_read_b64_tr_b16 v[84:85], v32 offset:4096\n"
    "v_mfma_f32_16x16x32_bf16 v[132:135], v[60:63], v[36:39], v[132:135]\n"
    "v_lshlrev_b32 v200, 16, v180\n"
    "ds_read_b64_tr_b16 v[86:87], v32 offset:4608\n"
    "v_mfma_f32_16x16x32_bf16 v[136:139], v[60:63], v[40:43], v[136:139]\n"
    "v_and_b32 v201, 0xffff0000, v180\n"
    "ds_read_b64_tr_b16 v[88:89], v33 offset:4096\n"
    "v_mfma_f32_16x16x32_bf16 v[156:159], v[60:63], v[168:171], v[156:159]\n"
    "v_lshlrev_b32 v202, 16, v181\n"
    "ds_read_b64_tr_b16 v[90:91], v33 offset:4608\n"
    "v_and_b32 v203, 0xffff0000, v181\n"
    "v_mfma_f32_16x16x32_bf16 v[140:143], v[64:67], v[36:39], v[140:143]\n"
    "ds_read_b64_tr_b16 v[92:93], v34 offset:4096\n"
    "v_lshlrev_b32 v204, 16, v182\n"
    "v_mfma_f32_16x16x32_bf16 v[144:147], v[64:67], v[40:43], v[144:147]\n"
    "ds_read_b64_tr_b16 v[94:95], v34 offset:4608\n"
    "v_and_b32 v205, 0xffff0000, v182\n"
    "v_mfma_f32_16x16x32_bf16 v[160:163], v[168:171], v[36:39], v[160:163]\n"
    "ds_read_b64_tr_b16 v[96:97], v35 offset:4096\n"
    "v_lshlrev_b32 v206, 16, v183\n"
    "v_mfma_f32_16x16x32_bf16 v[164:167], v[36:39], v[36:39], v[164:167]\n"
    "ds_read_b64_tr_b16 v[98:99], v35 offset:4608\n"
    "v_and_b32 v207, 0xffff0000, v183\n"
    "s_waitcnt lgkmcnt(0)\n"
    "s_waitcnt vmcnt(12)\n"
    "s_barrier\n"
    "v_mfma_f32_16x16x32_bf16 v[100:103], v[76:79], v[68:71], v[100:103]\n"
    "ds_read_b128 v[36:39], v20 offset:32768\n"
    "ds_read_b64_tr_b16 v[44:45], v30 offset:32768\n"
    "v_mfma_f32_16x16x32_bf16 v[104:107], v[76:79], v[72:75], v[104:107]\n"
    "ds_read_b64_tr_b16 v[46:47], v30 offset:33280\n"
    "v_mfma_f32_16x16x32_bf16 v[148:151], v[76:79], v[168:171], v[148:151]\n"
    "ds_read_b128 v[40:43], v22 offset:32768\n"
    "v_mfma_f32_16x16x32_bf16 v[108:111], v[80:83], v[68:71], v[108:111]\n"
    "ds_read_b64_tr_b16 v[48:49], v31 offset:32768\n"
    "v_mfma_f32_16x16x32_bf16 v[112:115], v[80:83], v[72:75], v[112:115]\n"
    "ds_read_b64_tr_b16 v[50:51], v31 offset:33280\n"
    "v_mfma_f32_16x16x32_bf16 v[116:119], v[84:87], v[68:71], v[116:119]\n"
    "ds_read_b64_tr_b16 v[52:53], v32 offset:32768\n"
    "s_add_u32 m0, s47, 49024\n"
    "v_mfma_f32_16x16x32_bf16 v[120:123], v[84:87], v[72:75], v[120:123]\n"
    "buffer_load_dwordx4 v14, s[16:19], s49 offen offset:128 lds\n"
    "v_mfma_f32_16x16x32_bf16 v[152:155], v[84:87], v[168:171], v[152:155]\n"
    "buffer_load_dwordx4 v15, s[16:19], s49 offen offset:1152 lds\n"
    "v_mfma_f32_16x16x32_bf16 v[124:127], v[88:91], v[68:71], v[124:127]\n"
    "ds_read_b64_tr_b16 v[54:55], v32 offset:33280\n"
    "v_mfma_f32_16x16x32_bf16 v[128:131], v[88:91], v[72:75], v[128:131]\n"
    "ds_read_b64_tr_b16 v[56:57], v33 offset:32768\n"
    "v_mfma_f32_16x16x32_bf16 v[132:135], v[92:95], v[68:71], v[132:135]\n"
    "ds_read_b64_tr_b16 v[58:59], v33 offset:33280\n"
    "v_mfma_f32_16x16x32_bf16 v[136:139], v[92:95], v[72:75], v[136:139]\n"
    "ds_read_b64_tr_b16 v[60:61], v34 offset:32768\n"
    "ds_read_b64_tr_b16 v[62:63], v34 offset:33280\n"
    "v_mfma_f32_16x16x32_bf16 v[156:159], v[92:95], v[168:171], v[156:159]\n"
    "ds_read_b64_tr_b16 v[64:65], v35 offset:32768\n"
    "v_mfma_f32_16x16x32_bf16 v[140:143], v[96:99], v[68:71], v[140:143]\n"
    "ds_read_b64_tr_b16 v[66:67], v35 offset:33280\n"
    "v_mfma_f32_16x16x32_bf16 v[144:147], v[96:99], v[72:75], v[144:147]\n"
    "s_add_u32 m0, s47, 57088\n"
    "v_mfma_f32_16x16x32_bf16 v[160:163], v[168:171], v[68:71], v[160:163]\n"
    "buffer_load_dwordx4 v14, s[16:19], s49 offen offset:256 lds\n"
    "v_mfma_f32_16x16x32_bf16 v[164:167], v[68:71], v[68:71], v[164:167]\n"
    "buffer_load_dwordx4 v15, s[16:19], s49 offen offset:1280 lds\n"
    "s_waitcnt lgkmcnt(10)\n"
    "v_mfma_f32_16x16x32_bf16 v[100:103], v[44:47], v[36:39], v[100:103]\n"
    "ds_read_b128 v[68:71], v21 offset:32768\n"
    "ds_read_b64_tr_b16 v[76:77], v30 offset:36864\n"
    "v_mfma_f32_16x16x32_bf16 v[104:107], v[44:47], v[40:43], v[104:107]\n"
    "ds_read_b64_tr_b16 v[78:79], v30 offset:37376\n"
    "v_mfma_f32_16x16x32_bf16 v[148:151], v[44:47], v[168:171], v[148:151]\n"
    "ds_read_b128 v[72:75], v23 offset:32768\n"
    "ds_read_b64_tr_b16 v[80:81], v31 offset:36864\n"
    "s_waitcnt lgkmcnt(5)\n"
    "v_mfma_f32_16x16x32_bf16 v[108:111], v[48:51], v[36:39], v[108:111]\n"
    "ds_read_b64_tr_b16 v[82:83], v31 offset:37376\n"
    "v_mfma_f32_16x16x32_bf16 v[112:115], v[48:51], v[40:43], v[112:115]\n"
    "ds_read_b64_tr_b16 v[84:85], v32 offset:36864\n"
    "ds_read_b64_tr_b16 v[86:87], v32 offset:37376\n"
    "v_mfma_f32_16x16x32_bf16 v[116:119], v[52:55], v[36:39], v[116:119]\n"
    "ds_read_b64_tr_b16 v[88:89], v33 offset:36864\n"
    "v_mfma_f32_16x16x32_bf16 v[120:123], v[52:55], v[40:43], v[120:123]\n"
    "ds_read_b64_tr_b16 v[90:91], v33 offset:37376\n"
    "ds_read_b64_tr_b16 v[92:93], v34 offset:36864\n"
    "v_mfma_f32_16x16x32_bf16 v[152:155], v[52:55], v[168:171], v[152:155]\n"
    "ds_read_b64_tr_b16 v[94:95], v34 offset:37376\n"
    "v_mfma_f32_16x16x32_bf16 v[124:127], v[56:59], v[36:39], v[124:127]\n"
    "ds_read_b64_tr_b16 v[96:97], v35 offset:36864\n"
    "ds_read_b64_tr_b16 v[98:99], v35 offset:37376\n"
    "v_mfma_f32_16x16x32_bf16 v[128:131], v[56:59], v[40:43], v[128:131]\n"
    "s_lshr_b64 s[8:9], s[92:93], 0\n"
    "v_mfma_f32_16x16x32_bf16 v[132:135], v[60:63], v[36:39], v[132:135]\n"
    "s_and_b32 s8, s8, 15\n"
    "s_mul_i32 s8, s8, 40960\n"
    "v_mfma_f32_16x16x32_bf16 v[136:139], v[60:63], v[40:43], v[136:139]\n"
    "s_add_u32 s11, s4, s8\n"
    "v_mfma_f32_16x16x32_bf16 v[156:159], v[60:63], v[168:171], v[156:159]\n"
    "s_add_u32 m0, s7, 131072\n"
    "v_mfma_f32_16x16x32_bf16 v[140:143], v[64:67], v[36:39], v[140:143]\n"
    "buffer_load_dwordx4 v251, s[24:27], s11 offen offset:0 lds\n"
    "buffer_load_dwordx4 v252, s[24:27], s11 offen offset:1024 lds\n"
    "v_mfma_f32_16x16x32_bf16 v[144:147], v[64:67], v[40:43], v[144:147]\n"
    "buffer_load_dwordx4 v253, s[24:27], s11 offen offset:2048 lds\n"
    "buffer_load_dwordx4 v254, s[24:27], s11 offen offset:3072 lds\n"
    "v_mfma_f32_16x16x32_bf16 v[160:163], v[168:171], v[36:39], v[160:163]\n"
    "s_add_u32 m0, s7, 135168\n"
    "v_mfma_f32_16x16x32_bf16 v[164:167], v[36:39], v[36:39], v[164:167]\n"
    "buffer_load_dwordx4 v255, s[24:27], s11 offen offset:0 lds\n"
    "s_waitcnt lgkmcnt(0)\n"
    "s_waitcnt vmcnt(13)\n"
    "s_barrier\n"
    "v_mfma_f32_16x16x32_bf16 v[100:103], v[76:79], v[68:71], v[100:103]\n"
    "ds_read_b128 v[36:39], v16 offset:0\n"
    "ds_read_b64_tr_b16 v[44:45], v24 offset:0\n"
    "v_mfma_f32_16x16x32_bf16 v[104:107], v[76:79], v[72:75], v[104:107]\n"
    "ds_read_b64_tr_b16 v[46:47], v24 offset:512\n"
    "v_mfma_f32_16x16x32_bf16 v[148:151], v[76:79], v[168:171], v[148:151]\n"
    "ds_read_b128 v[40:43], v18 offset:0\n"
    "ds_read_b64_tr_b16 v[48:49], v25 offset:0\n"
    "v_mfma_f32_16x16x32_bf16 v[108:111], v[80:83], v[68:71], v[108:111]\n"
    "ds_read_b64_tr_b16 v[50:51], v25 offset:512\n"
    "v_mfma_f32_16x16x32_bf16 v[112:115], v[80:83], v[72:75], v[112:115]\n"
    "ds_read_b64_tr_b16 v[52:53], v26 offset:0\n"
    "ds_read_b64_tr_b16 v[54:55], v26 offset:512\n"
    "v_mfma_f32_16x16x32_bf16 v[116:119], v[84:87], v[68:71], v[116:119]\n"
    "ds_read_b64_tr_b16 v[56:57], v27 offset:0\n"
    "v_mfma_f32_16x16x32_bf16 v[120:123], v[84:87], v[72:75], v[120:123]\n"
    "ds_read_b64_tr_b16 v[58:59], v27 offset:512\n"
    "ds_read_b64_tr_b16 v[60:61], v28 offset:0\n"
    "v_mfma_f32_16x16x32_bf16 v[152:155], v[84:87], v[168:171], v[152:155]\n"
    "ds_read_b64_tr_b16 v[62:63], v28 offset:512\n"
    "v_mfma_f32_16x16x32_bf16 v[124:127], v[88:91], v[68:71], v[124:127]\n"
    "ds_read_b64_tr_b16 v[64:65], v29 offset:0\n"
    "ds_read_b64_tr_b16 v[66:67], v29 offset:512\n"
    "v_mfma_f32_16x16x32_bf16 v[128:131], v[88:91], v[72:75], v[128:131]\n"
    "s_lshr_b64 s[8:9], s[92:93], 4\n"
    "v_mfma_f32_16x16x32_bf16 v[132:135], v[92:95], v[68:71], v[132:135]\n"
    "s_and_b32 s8, s8, 15\n"
    "s_mul_i32 s8, s8, 40960\n"
    "v_mfma_f32_16x16x32_bf16 v[136:139], v[92:95], v[72:75], v[136:139]\n"
    "s_add_u32 s11, s4, s8\n"
    "v_mfma_f32_16x16x32_bf16 v[156:159], v[92:95], v[168:171], v[156:159]\n"
    "s_add_u32 m0, s7, 65536\n"
    "v_mfma_f32_16x16x32_bf16 v[140:143], v[96:99], v[68:71], v[140:143]\n"
    "buffer_load_dwordx4 v251, s[24:27], s11 offen offset:0 lds\n"
    "buffer_load_dwordx4 v252, s[24:27], s11 offen offset:1024 lds\n"
    "v_mfma_f32_16x16x32_bf16 v[144:147], v[96:99], v[72:75], v[144:147]\n"
    "buffer_load_dwordx4 v253, s[24:27], s11 offen offset:2048 lds\n"
    "buffer_load_dwordx4 v254, s[24:27], s11 offen offset:3072 lds\n"
    "v_mfma_f32_16x16x32_bf16 v[160:163], v[168:171], v[68:71], v[160:163]\n"
    "s_add_u32 m0, s7, 69632\n"
    "v_mfma_f32_16x16x32_bf16 v[164:167], v[68:71], v[68:71], v[164:167]\n"
    "buffer_load_dwordx4 v255, s[24:27], s11 offen offset:0 lds\n"
    "s_waitcnt lgkmcnt(10)\n"
    "v_mfma_f32_16x16x32_bf16 v[100:103], v[44:47], v[36:39], v[100:103]\n"
    "ds_read_b128 v[68:71], v17 offset:0\n"
    "ds_read_b64_tr_b16 v[76:77], v24 offset:4096\n"
    "v_mfma_f32_16x16x32_bf16 v[104:107], v[44:47], v[40:43], v[104:107]\n"
    "ds_read_b64_tr_b16 v[78:79], v24 offset:4608\n"
    "v_mfma_f32_16x16x32_bf16 v[148:151], v[44:47], v[168:171], v[148:151]\n"
    "ds_read_b128 v[72:75], v19 offset:0\n"
    "ds_read_b64_tr_b16 v[80:81], v25 offset:4096\n"
    "s_waitcnt lgkmcnt(5)\n"
    "v_mfma_f32_16x16x32_bf16 v[108:111], v[48:51], v[36:39], v[108:111]\n"
    "ds_read_b64_tr_b16 v[82:83], v25 offset:4608\n"
    "v_mfma_f32_16x16x32_bf16 v[112:115], v[48:51], v[40:43], v[112:115]\n"
    "ds_read_b64_tr_b16 v[84:85], v26 offset:4096\n"
    "ds_read_b64_tr_b16 v[86:87], v26 offset:4608\n"
    "v_mfma_f32_16x16x32_bf16 v[116:119], v[52:55], v[36:39], v[116:119]\n"
    "ds_read_b64_tr_b16 v[88:89], v27 offset:4096\n"
    "v_mfma_f32_16x16x32_bf16 v[120:123], v[52:55], v[40:43], v[120:123]\n"
    "ds_read_b64_tr_b16 v[90:91], v27 offset:4608\n"
    "ds_read_b64_tr_b16 v[92:93], v28 offset:4096\n"
    "v_mfma_f32_16x16x32_bf16 v[152:155], v[52:55], v[168:171], v[152:155]\n"
    "ds_read_b64_tr_b16 v[94:95], v28 offset:4608\n"
    "v_mfma_f32_16x16x32_bf16 v[124:127], v[56:59], v[36:39], v[124:127]\n"
    "ds_read_b64_tr_b16 v[96:97], v29 offset:4096\n"
    "ds_read_b64_tr_b16 v[98:99], v29 offset:4608\n"
    "v_mfma_f32_16x16x32_bf16 v[128:131], v[56:59], v[40:43], v[128:131]\n"
    "s_lshr_b64 s[8:9], s[92:93], 8\n"
    "v_mfma_f32_16x16x32_bf16 v[132:135], v[60:63], v[36:39], v[132:135]\n"
    "s_and_b32 s8, s8, 15\n"
    "s_mul_i32 s8, s8, 40960\n"
    "v_mfma_f32_16x16x32_bf16 v[136:139], v[60:63], v[40:43], v[136:139]\n"
    "s_add_u32 s11, s4, s8\n"
    "v_mfma_f32_16x16x32_bf16 v[156:159], v[60:63], v[168:171], v[156:159]\n"
    "s_add_u32 m0, s7, 98304\n"
    "v_mfma_f32_16x16x32_bf16 v[140:143], v[64:67], v[36:39], v[140:143]\n"
    "buffer_load_dwordx4 v251, s[24:27], s11 offen offset:0 lds\n"
    "buffer_load_dwordx4 v252, s[24:27], s11 offen offset:1024 lds\n"
    "v_mfma_f32_16x16x32_bf16 v[144:147], v[64:67], v[40:43], v[144:147]\n"
    "buffer_load_dwordx4 v253, s[24:27], s11 offen offset:2048 lds\n"
    "buffer_load_dwordx4 v254, s[24:27], s11 offen offset:3072 lds\n"
    "v_mfma_f32_16x16x32_bf16 v[160:163], v[168:171], v[36:39], v[160:163]\n"
    "s_add_u32 m0, s7, 102400\n"
    "v_mfma_f32_16x16x32_bf16 v[164:167], v[36:39], v[36:39], v[164:167]\n"
    "buffer_load_dwordx4 v255, s[24:27], s11 offen offset:0 lds\n"
    "s_waitcnt lgkmcnt(0)\n"
    "s_waitcnt vmcnt(15)\n"
    "s_barrier\n"
    "v_mfma_f32_16x16x32_bf16 v[100:103], v[76:79], v[68:71], v[100:103]\n"
    "ds_read_b128 v[36:39], v16 offset:32768\n"
    "ds_read_b64_tr_b16 v[44:45], v24 offset:32768\n"
    "v_mfma_f32_16x16x32_bf16 v[104:107], v[76:79], v[72:75], v[104:107]\n"
    "ds_read_b64_tr_b16 v[46:47], v24 offset:33280\n"
    "v_mfma_f32_16x16x32_bf16 v[148:151], v[76:79], v[168:171], v[148:151]\n"
    "ds_read_b128 v[40:43], v18 offset:32768\n"
    "ds_read_b64_tr_b16 v[48:49], v25 offset:32768\n"
    "v_mfma_f32_16x16x32_bf16 v[108:111], v[80:83], v[68:71], v[108:111]\n"
    "ds_read_b64_tr_b16 v[50:51], v25 offset:33280\n"
    "v_mfma_f32_16x16x32_bf16 v[112:115], v[80:83], v[72:75], v[112:115]\n"
    "ds_read_b64_tr_b16 v[52:53], v26 offset:32768\n"
    "ds_read_b64_tr_b16 v[54:55], v26 offset:33280\n"
    "v_mfma_f32_16x16x32_bf16 v[116:119], v[84:87], v[68:71], v[116:119]\n"
    "ds_read_b64_tr_b16 v[56:57], v27 offset:32768\n"
    "v_mfma_f32_16x16x32_bf16 v[120:123], v[84:87], v[72:75], v[120:123]\n"
    "ds_read_b64_tr_b16 v[58:59], v27 offset:33280\n"
    "ds_read_b64_tr_b16 v[60:61], v28 offset:32768\n"
    "v_mfma_f32_16x16x32_bf16 v[152:155], v[84:87], v[168:171], v[152:155]\n"
    "ds_read_b64_tr_b16 v[62:63], v28 offset:33280\n"
    "v_mfma_f32_16x16x32_bf16 v[124:127], v[88:91], v[68:71], v[124:127]\n"
    "ds_read_b64_tr_b16 v[64:65], v29 offset:32768\n"
    "ds_read_b64_tr_b16 v[66:67], v29 offset:33280\n"
    "v_mfma_f32_16x16x32_bf16 v[128:131], v[88:91], v[72:75], v[128:131]\n"
    "s_lshr_b64 s[8:9], s[92:93], 12\n"
    "v_mfma_f32_16x16x32_bf16 v[132:135], v[92:95], v[68:71], v[132:135]\n"
    "s_and_b32 s8, s8, 15\n"
    "s_mul_i32 s8, s8, 40960\n"
    "v_mfma_f32_16x16x32_bf16 v[136:139], v[92:95], v[72:75], v[136:139]\n"
    "s_add_u32 s11, s4, s8\n"
    "v_mfma_f32_16x16x32_bf16 v[156:159], v[92:95], v[168:171], v[156:159]\n"
    "s_add_u32 m0, s7, 0\n"
    "v_mfma_f32_16x16x32_bf16 v[140:143], v[96:99], v[68:71], v[140:143]\n"
    "buffer_load_dwordx4 v251, s[24:27], s11 offen offset:0 lds\n"
    "buffer_load_dwordx4 v252, s[24:27], s11 offen offset:1024 lds\n"
    "v_mfma_f32_16x16x32_bf16 v[144:147], v[96:99], v[72:75], v[144:147]\n"
    "buffer_load_dwordx4 v253, s[24:27], s11 offen offset:2048 lds\n"
    "buffer_load_dwordx4 v254, s[24:27], s11 offen offset:3072 lds\n"
    "v_mfma_f32_16x16x32_bf16 v[160:163], v[168:171], v[68:71], v[160:163]\n"
    "s_add_u32 m0, s7, 4096\n"
    "v_mfma_f32_16x16x32_bf16 v[164:167], v[68:71], v[68:71], v[164:167]\n"
    "buffer_load_dwordx4 v255, s[24:27], s11 offen offset:0 lds\n"
    "s_waitcnt lgkmcnt(10)\n"
    "v_mfma_f32_16x16x32_bf16 v[100:103], v[44:47], v[36:39], v[100:103]\n"
    "ds_read_b128 v[68:71], v17 offset:32768\n"
    "v_mfma_f32_16x16x32_bf16 v[104:107], v[44:47], v[40:43], v[104:107]\n"
    "ds_read_b64_tr_b16 v[76:77], v24 offset:36864\n"
    "v_mfma_f32_16x16x32_bf16 v[148:151], v[44:47], v[168:171], v[148:151]\n"
    "ds_read_b64_tr_b16 v[78:79], v24 offset:37376\n"
    "s_waitcnt lgkmcnt(3)\n"
    "v_mfma_f32_16x16x32_bf16 v[108:111], v[48:51], v[36:39], v[108:111]\n"
    "ds_read_b128 v[72:75], v19 offset:32768\n"
    "v_mfma_f32_16x16x32_bf16 v[112:115], v[48:51], v[40:43], v[112:115]\n"
    "ds_read_b64_tr_b16 v[80:81], v25 offset:36864\n"
    "v_mfma_f32_16x16x32_bf16 v[116:119], v[52:55], v[36:39], v[116:119]\n"
    "v_mfma_f32_16x16x32_bf16 v[120:123], v[52:55], v[40:43], v[120:123]\n"
    "ds_read_b64_tr_b16 v[82:83], v25 offset:37376\n"
    "v_mfma_f32_16x16x32_bf16 v[152:155], v[52:55], v[168:171], v[152:155]\n"
    "ds_read_b64_tr_b16 v[84:85], v26 offset:36864\n"
    "v_mfma_f32_16x16x32_bf16 v[124:127], v[56:59], v[36:39], v[124:127]\n"
    "ds_read_b64_tr_b16 v[86:87], v26 offset:37376\n"
    "v_mfma_f32_16x16x32_bf16 v[128:131], v[56:59], v[40:43], v[128:131]\n"
    "ds_read_b64_tr_b16 v[88:89], v27 offset:36864\n"
    "v_mfma_f32_16x16x32_bf16 v[132:135], v[60:63], v[36:39], v[132:135]\n"
    "ds_read_b64_tr_b16 v[90:91], v27 offset:37376\n"
    "v_mfma_f32_16x16x32_bf16 v[136:139], v[60:63], v[40:43], v[136:139]\n"
    "v_mfma_f32_16x16x32_bf16 v[156:159], v[60:63], v[168:171], v[156:159]\n"
    "ds_read_b64_tr_b16 v[92:93], v28 offset:36864\n"
    "v_mfma_f32_16x16x32_bf16 v[140:143], v[64:67], v[36:39], v[140:143]\n"
    "ds_read_b64_tr_b16 v[94:95], v28 offset:37376\n"
    "v_mfma_f32_16x16x32_bf16 v[144:147], v[64:67], v[40:43], v[144:147]\n"
    "ds_read_b64_tr_b16 v[96:97], v29 offset:36864\n"
    "v_mfma_f32_16x16x32_bf16 v[160:163], v[168:171], v[36:39], v[160:163]\n"
    "ds_read_b64_tr_b16 v[98:99], v29 offset:37376\n"
    "v_mfma_f32_16x16x32_bf16 v[164:167], v[36:39], v[36:39], v[164:167]\n"
    "s_waitcnt lgkmcnt(0)\n"
    "v_mfma_f32_16x16x32_bf16 v[160:163], v[168:171], v[68:71], v[160:163]\n"
    "v_mfma_f32_16x16x32_bf16 v[164:167], v[68:71], v[68:71], v[164:167]\n"
    "v_mfma_f32_16x16x32_bf16 v[148:151], v[76:79], v[168:171], v[148:151]\n"
    "v_mfma_f32_16x16x32_bf16 v[152:155], v[84:87], v[168:171], v[152:155]\n"
    "v_mfma_f32_16x16x32_bf16 v[156:159], v[92:95], v[168:171], v[156:159]\n"
    "v_mfma_f32_16x16x32_bf16 v[100:103], v[76:79], v[68:71], v[100:103]\n"
    "v_mfma_f32_16x16x32_bf16 v[104:107], v[76:79], v[72:75], v[104:107]\n"
    "v_mfma_f32_16x16x32_bf16 v[108:111], v[80:83], v[68:71], v[108:111]\n"
    "v_and_b32 v1, 3, v240\n"
    "v_cmp_eq_u32 vcc, 1, v1\n"
    "v_mfma_f32_16x16x32_bf16 v[112:115], v[80:83], v[72:75], v[112:115]\n"
    "s_nop 0\n"
    "v_cndmask_b32 v2, v164, v165, vcc\n"
    "v_cmp_eq_u32 vcc, 2, v1\n"
    "v_mfma_f32_16x16x32_bf16 v[116:119], v[84:87], v[68:71], v[116:119]\n"
    "s_nop 0\n"
    "v_cndmask_b32 v2, v2, v166, vcc\n"
    "v_cmp_eq_u32 vcc, 3, v1\n"
    "v_mfma_f32_16x16x32_bf16 v[120:123], v[84:87], v[72:75], v[120:123]\n"
    "s_nop 0\n"
    "v_cndmask_b32 v2, v2, v167, vcc\n"
    "v_mul_f32 v3, 0x3acccccd, v160\n"
    "v_mfma_f32_16x16x32_bf16 v[124:127], v[88:91], v[68:71], v[124:127]\n"
    "v_mul_f32 v4, v3, v3\n"
    "v_mov_b32 v1, 0x3acccccd\n"
    "v_mfma_f32_16x16x32_bf16 v[128:131], v[88:91], v[72:75], v[128:131]\n"
    "v_fma_f32 v2, v2, v1, -v4\n"
    "v_max_f32 v2, 0, v2\n"
    "v_mfma_f32_16x16x32_bf16 v[132:135], v[92:95], v[68:71], v[132:135]\n"
    "v_add_f32 v2, 0x3727c5ac, v2\n"
    "v_rsq_f32 v4, v2\n"
    "v_mfma_f32_16x16x32_bf16 v[136:139], v[92:95], v[72:75], v[136:139]\n"
    "v_mul_f32 v5, v3, v4\n"
    "v_xor_b32 v5, 0x80000000, v5\n"
    "v_mfma_f32_16x16x32_bf16 v[140:143], v[96:99], v[68:71], v[140:143]\n"
    "v_mfma_f32_16x16x32_bf16 v[144:147], v[96:99], v[72:75], v[144:147]\n"
    "s_mov_b32 exec_lo, 0x00010001\n"
    "s_mov_b32 exec_hi, 0x00010001\n"
    "ds_write_b128 v224, v[148:151] offset:0\n"
    "ds_write_b128 v224, v[152:155] offset:64\n"
    "ds_write_b128 v224, v[156:159] offset:128\n"
    "s_mov_b32 exec_lo, 0x00f0000f\n"
    "s_mov_b32 exec_hi, 0xf0000f00\n"
    "ds_write_b64 v226, v[4:5]\n"
    "s_mov_b64 exec, -1\n"
    "s_waitcnt lgkmcnt(0)\n"
    "s_barrier\n"
    "ds_read_b128 v[208:211], v225 offset:0\n"
    "ds_read_b128 v[212:215], v225 offset:64\n"
    "ds_read_b128 v[216:219], v225 offset:128\n"
    "ds_read_b64 v[220:221], v226\n"
    "ds_read_b64 v[222:223], v227\n"
    "s_lshr_b64 s[8:9], s[92:93], 16\n"
    "s_and_b32 s8, s8, 15\n"
    "s_mul_i32 s8, s8, 40960\n"
    "s_add_u32 s11, s4, s8\n"
    "s_add_u32 m0, s7, 32768\n"
    "s_nop 0\n"
    "buffer_load_dwordx4 v251, s[24:27], s11 offen offset:0 lds\n"
    "buffer_load_dwordx4 v252, s[24:27], s11 offen offset:1024 lds\n"
    "buffer_load_dwordx4 v253, s[24:27], s11 offen offset:2048 lds\n"
    "buffer_load_dwordx4 v254, s[24:27], s11 offen offset:3072 lds\n"
    "s_add_u32 m0, s7, 36864\n"
    "s_nop 0\n"
    "buffer_load_dwordx4 v255, s[24:27], s11 offen offset:0 lds\n"
    "s_waitcnt lgkmcnt(0)\n"
    "v_pk_fma_f32 v[44:45], v[148:149], v[220:221], v[184:185] op_sel:[0,1,0] op_sel_hi:[1,1,1]\n"
    "v_pk_fma_f32 v[46:47], v[150:151], v[220:221], v[186:187] op_sel:[0,1,0] op_sel_hi:[1,1,1]\n"
    "v_pk_fma_f32 v[48:49], v[208:209], v[220:221], v[188:189] op_sel:[0,1,0] op_sel_hi:[1,1,1]\n"
    "v_pk_fma_f32 v[50:51], v[210:211], v[220:221], v[190:191] op_sel:[0,1,0] op_sel_hi:[1,1,1]\n"
    "v_pk_fma_f32 v[36:37], v[100:101], v[220:221], v[44:45] op_sel_hi:[1,0,1]\n"
    "v_pk_fma_f32 v[38:39], v[102:103], v[220:221], v[46:47] op_sel_hi:[1,0,1]\n"
    "v_pk_fma_f32 v[40:41], v[108:109], v[220:221], v[48:49] op_sel_hi:[1,0,1]\n"
    "v_pk_fma_f32 v[42:43], v[110:111], v[220:221], v[50:51] op_sel_hi:[1,0,1]\n"
    "v_pk_mul_f32 v[44:45], v[36:37], v[36:37]\n"
    "v_pk_mul_f32 v[46:47], v[38:39], v[38:39]\n"
    "v_pk_mul_f32 v[48:49], v[40:41], v[40:41]\n"
    "v_pk_mul_f32 v[50:51], v[42:43], v[42:43]\n"
    "v_pk_fma_f32 v[44:45], v[44:45], s[54:55], s[54:55] op_sel:[0,0,1] op_sel_hi:[1,0,1]\n"
    "v_pk_fma_f32 v[46:47], v[46:47], s[54:55], s[54:55] op_sel:[0,0,1] op_sel_hi:[1,0,1]\n"
    "v_pk_fma_f32 v[48:49], v[48:49], s[54:55], s[54:55] op_sel:[0,0,1] op_sel_hi:[1,0,1]\n"
    "v_pk_fma_f32 v[50:51], v[50:51], s[54:55], s[54:55] op_sel:[0,0,1] op_sel_hi:[1,0,1]\n"
    "v_pk_mul_f32 v[44:45], v[36:37], v[44:45]\n"
    "v_pk_mul_f32 v[46:47], v[38:39], v[46:47]\n"
    "v_pk_mul_f32 v[48:49], v[40:41], v[48:49]\n"
    "v_pk_mul_f32 v[50:51], v[42:43], v[50:51]\n"
    "v_exp_f32 v44, v44\n"
    "v_exp_f32 v45, v45\n"
    "v_exp_f32 v46, v46\n"
    "v_exp_f32 v47, v47\n"
    "v_exp_f32 v48, v48\n"
    "v_exp_f32 v49, v49\n"
    "v_exp_f32 v50, v50\n"
    "v_exp_f32 v51, v51\n"
    "v_pk_add_f32 v[44:45], v[44:45], 1.0 op_sel_hi:[1,0]\n"
    "v_pk_add_f32 v[46:47], v[46:47], 1.0 op_sel_hi:[1,0]\n"
    "v_pk_add_f32 v[48:49], v[48:49], 1.0 op_sel_hi:[1,0]\n"
    "v_pk_add_f32 v[50:51], v[50:51], 1.0 op_sel_hi:[1,0]\n"
    "v_rcp_f32 v44, v44\n"
    "v_rcp_f32 v45, v45\n"
    "v_rcp_f32 v46, v46\n"
    "v_rcp_f32 v47, v47\n"
    "v_rcp_f32 v48, v48\n"
    "v_rcp_f32 v49, v49\n"
    "v_rcp_f32 v50, v50\n"
    "v_rcp_f32 v51, v51\n"
    "v_pk_mul_f32 v[36:37], v[36:37], v[44:45]\n"
    "v_pk_mul_f32 v[38:39], v[38:39], v[46:47]\n"
    "v_pk_mul_f32 v[40:41], v[40:41], v[48:49]\n"
    "v_pk_mul_f32 v[42:43], v[42:43], v[50:51]\n"
    "v_cvt_pk_bf16_f32 v44, v36, v37\n"
    "v_cvt_pk_bf16_f32 v45, v38, v39\n"
    "v_cvt_pk_bf16_f32 v46, v40, v41\n"
    "v_cvt_pk_bf16_f32 v47, v42, v43\n"
    "v_cndmask_b32 v68, v44, v46, s[62:63]\n"
    "v_cndmask_b32 v69, v45, v47, s[62:63]\n"
    "v_cndmask_b32 v70, v46, v44, s[62:63]\n"
    "v_cndmask_b32 v71, v47, v45, s[62:63]\n"
    "s_add_u32 s56, s64, 0\n"
    "v_lshlrev_b32 v241, 4, v240\n"
    "buffer_store_dwordx4 v[68:71], v241, s[36:39], s56 offen sc1\n"
    "s_waitcnt vmcnt(21)\n"
    "ds_read_b64_tr_b16 a[0:1], v250 offset:0\n"
    "ds_read_b64_tr_b16 a[2:3], v250 offset:2560\n"
    "ds_read_b64_tr_b16 a[4:5], v250 offset:32\n"
    "ds_read_b64_tr_b16 a[6:7], v250 offset:2592\n"
    "ds_read_b64_tr_b16 a[8:9], v250 offset:64\n"
    "ds_read_b64_tr_b16 a[10:11], v250 offset:2624\n"
    "ds_read_b64_tr_b16 a[12:13], v250 offset:96\n"
    "ds_read_b64_tr_b16 a[14:15], v250 offset:2656\n"
    "ds_read_b64_tr_b16 a[16:17], v250 offset:128\n"
    "ds_read_b64_tr_b16 a[18:19], v250 offset:2688\n"
    "s_waitcnt lgkmcnt(0)\n"
    "s_lshr_b64 s[8:9], s[92:93], 20\n"
    "s_and_b32 s8, s8, 15\n"
    "s_mul_i32 s8, s8, 40960\n"
    "s_add_u32 s11, s4, s8\n"
    "s_add_u32 m0, s7, 131072\n"
    "s_nop 0\n"
    "buffer_load_dwordx4 v251, s[24:27], s11 offen offset:0 lds\n"
    "buffer_load_dwordx4 v252, s[24:27], s11 offen offset:1024 lds\n"
    "buffer_load_dwordx4 v253, s[24:27], s11 offen offset:2048 lds\n"
    "buffer_load_dwordx4 v254, s[24:27], s11 offen offset:3072 lds\n"
    "s_add_u32 m0, s7, 135168\n"
    "s_nop 0\n"
    "buffer_load_dwordx4 v255, s[24:27], s11 offen offset:0 lds\n"
    "v_pk_fma_f32 v[60:61], v[152:153], v[220:221], v[192:193] op_sel:[0,1,0] op_sel_hi:[1,1,1]\n"
    "v_pk_fma_f32 v[62:63], v[154:155], v[220:221], v[194:195] op_sel:[0,1,0] op_sel_hi:[1,1,1]\n"
    "v_pk_fma_f32 v[64:65], v[212:213], v[220:221], v[196:197] op_sel:[0,1,0] op_sel_hi:[1,1,1]\n"
    "v_pk_fma_f32 v[66:67], v[214:215], v[220:221], v[198:199] op_sel:[0,1,0] op_sel_hi:[1,1,1]\n"
    "v_pk_fma_f32 v[52:53], v[116:117], v[220:221], v[60:61] op_sel_hi:[1,0,1]\n"
    "v_pk_fma_f32 v[54:55], v[118:119], v[220:221], v[62:63] op_sel_hi:[1,0,1]\n"
    "v_pk_fma_f32 v[56:57], v[124:125], v[220:221], v[64:65] op_sel_hi:[1,0,1]\n"
    "v_pk_fma_f32 v[58:59], v[126:127], v[220:221], v[66:67] op_sel_hi:[1,0,1]\n"
    "v_pk_mul_f32 v[60:61], v[52:53], v[52:53]\n"
    "v_pk_mul_f32 v[62:63], v[54:55], v[54:55]\n"
    "v_pk_mul_f32 v[64:65], v[56:57], v[56:57]\n"
    "v_pk_mul_f32 v[66:67], v[58:59], v[58:59]\n"
    "v_pk_fma_f32 v[60:61], v[60:61], s[54:55], s[54:55] op_sel:[0,0,1] op_sel_hi:[1,0,1]\n"
    "v_pk_fma_f32 v[62:63], v[62:63], s[54:55], s[54:55] op_sel:[0,0,1] op_sel_hi:[1,0,1]\n"
    "v_pk_fma_f32 v[64:65], v[64:65], s[54:55], s[54:55] op_sel:[0,0,1] op_sel_hi:[1,0,1]\n"
    "v_pk_fma_f32 v[66:67], v[66:67], s[54:55], s[54:55] op_sel:[0,0,1] op_sel_hi:[1,0,1]\n"
    "v_pk_mul_f32 v[60:61], v[52:53], v[60:61]\n"
    "v_pk_mul_f32 v[62:63], v[54:55], v[62:63]\n"
    "v_pk_mul_f32 v[64:65], v[56:57], v[64:65]\n"
    "v_pk_mul_f32 v[66:67], v[58:59], v[66:67]\n"
    "v_exp_f32 v60, v60\n"
    "v_exp_f32 v61, v61\n"
    "v_exp_f32 v62, v62\n"
    "v_exp_f32 v63, v63\n"
    "v_exp_f32 v64, v64\n"
    "v_exp_f32 v65, v65\n"
    "v_exp_f32 v66, v66\n"
    "v_exp_f32 v67, v67\n"
    "v_pk_add_f32 v[60:61], v[60:61], 1.0 op_sel_hi:[1,0]\n"
    "v_pk_add_f32 v[62:63], v[62:63], 1.0 op_sel_hi:[1,0]\n"
    "v_pk_add_f32 v[64:65], v[64:65], 1.0 op_sel_hi:[1,0]\n"
    "v_pk_add_f32 v[66:67], v[66:67], 1.0 op_sel_hi:[1,0]\n"
    "v_rcp_f32 v60, v60\n"
    "v_rcp_f32 v61, v61\n"
    "v_rcp_f32 v62, v62\n"
    "v_rcp_f32 v63, v63\n"
    "v_rcp_f32 v64, v64\n"
    "v_rcp_f32 v65, v65\n"
    "v_rcp_f32 v66, v66\n"
    "v_rcp_f32 v67, v67\n"
    "v_pk_mul_f32 v[52:53], v[52:53], v[60:61]\n"
    "v_pk_mul_f32 v[54:55], v[54:55], v[62:63]\n"
    "v_pk_mul_f32 v[56:57], v[56:57], v[64:65]\n"
    "v_pk_mul_f32 v[58:59], v[58:59], v[66:67]\n"
    "v_cvt_pk_bf16_f32 v60, v52, v53\n"
    "v_cvt_pk_bf16_f32 v61, v54, v55\n"
    "v_cvt_pk_bf16_f32 v62, v56, v57\n"
    "v_cvt_pk_bf16_f32 v63, v58, v59\n"
    "v_cndmask_b32 v72, v60, v62, s[62:63]\n"
    "v_cndmask_b32 v73, v61, v63, s[62:63]\n"
    "v_cndmask_b32 v74, v62, v60, s[62:63]\n"
    "v_cndmask_b32 v75, v63, v61, s[62:63]\n"
    "s_add_u32 s57, s64, 4096\n"
    "buffer_store_dwordx4 v[72:75], v241, s[36:39], s57 offen sc1\n"
    "s_waitcnt vmcnt(22)\n"
    "ds_read_b64_tr_b16 a[20:21], v249 offset:0\n"
    "ds_read_b64_tr_b16 a[22:23], v249 offset:2560\n"
    "ds_read_b64_tr_b16 a[24:25], v249 offset:32\n"
    "ds_read_b64_tr_b16 a[26:27], v249 offset:2592\n"
    "ds_read_b64_tr_b16 a[28:29], v249 offset:64\n"
    "ds_read_b64_tr_b16 a[30:31], v249 offset:2624\n"
    "ds_read_b64_tr_b16 a[32:33], v249 offset:96\n"
    "ds_read_b64_tr_b16 a[34:35], v249 offset:2656\n"
    "ds_read_b64_tr_b16 a[36:37], v249 offset:128\n"
    "ds_read_b64_tr_b16 a[38:39], v249 offset:2688\n"
    "s_waitcnt lgkmcnt(0)\n"
    "s_lshr_b64 s[8:9], s[92:93], 24\n"
    "s_and_b32 s8, s8, 15\n"
    "s_mul_i32 s8, s8, 40960\n"
    "s_add_u32 s11, s4, s8\n"
    "s_add_u32 m0, s7, 65536\n"
    "s_nop 0\n"
    "buffer_load_dwordx4 v251, s[24:27], s11 offen offset:0 lds\n"
    "buffer_load_dwordx4 v252, s[24:27], s11 offen offset:1024 lds\n"
    "buffer_load_dwordx4 v253, s[24:27], s11 offen offset:2048 lds\n"
    "buffer_load_dwordx4 v254, s[24:27], s11 offen offset:3072 lds\n"
    "s_add_u32 m0, s7, 69632\n"
    "s_nop 0\n"
    "buffer_load_dwordx4 v255, s[24:27], s11 offen offset:0 lds\n"
    "v_pk_fma_f32 v[44:45], v[156:157], v[220:221], v[200:201] op_sel:[0,1,0] op_sel_hi:[1,1,1]\n"
    "v_pk_fma_f32 v[46:47], v[158:159], v[220:221], v[202:203] op_sel:[0,1,0] op_sel_hi:[1,1,1]\n"
    "v_pk_fma_f32 v[48:49], v[216:217], v[220:221], v[204:205] op_sel:[0,1,0] op_sel_hi:[1,1,1]\n"
    "v_pk_fma_f32 v[50:51], v[218:219], v[220:221], v[206:207] op_sel:[0,1,0] op_sel_hi:[1,1,1]\n"
    "v_pk_fma_f32 v[36:37], v[132:133], v[220:221], v[44:45] op_sel_hi:[1,0,1]\n"
    "v_pk_fma_f32 v[38:39], v[134:135], v[220:221], v[46:47] op_sel_hi:[1,0,1]\n"
    "v_pk_fma_f32 v[40:41], v[140:141], v[220:221], v[48:49] op_sel_hi:[1,0,1]\n"
    "v_pk_fma_f32 v[42:43], v[142:143], v[220:221], v[50:51] op_sel_hi:[1,0,1]\n"
    "v_pk_mul_f32 v[44:45], v[36:37], v[36:37]\n"
    "v_pk_mul_f32 v[46:47], v[38:39], v[38:39]\n"
    "v_pk_mul_f32 v[48:49], v[40:41], v[40:41]\n"
    "v_pk_mul_f32 v[50:51], v[42:43], v[42:43]\n"
    "v_pk_fma_f32 v[44:45], v[44:45], s[54:55], s[54:55] op_sel:[0,0,1] op_sel_hi:[1,0,1]\n"
    "v_pk_fma_f32 v[46:47], v[46:47], s[54:55], s[54:55] op_sel:[0,0,1] op_sel_hi:[1,0,1]\n"
    "v_pk_fma_f32 v[48:49], v[48:49], s[54:55], s[54:55] op_sel:[0,0,1] op_sel_hi:[1,0,1]\n"
    "v_pk_fma_f32 v[50:51], v[50:51], s[54:55], s[54:55] op_sel:[0,0,1] op_sel_hi:[1,0,1]\n"
    "v_pk_mul_f32 v[44:45], v[36:37], v[44:45]\n"
    "v_pk_mul_f32 v[46:47], v[38:39], v[46:47]\n"
    "v_pk_mul_f32 v[48:49], v[40:41], v[48:49]\n"
    "v_pk_mul_f32 v[50:51], v[42:43], v[50:51]\n"
    "v_exp_f32 v44, v44\n"
    "v_exp_f32 v45, v45\n"
    "v_exp_f32 v46, v46\n"
    "v_exp_f32 v47, v47\n"
    "v_exp_f32 v48, v48\n"
    "v_exp_f32 v49, v49\n"
    "v_exp_f32 v50, v50\n"
    "v_exp_f32 v51, v51\n"
    "v_pk_add_f32 v[44:45], v[44:45], 1.0 op_sel_hi:[1,0]\n"
    "v_pk_add_f32 v[46:47], v[46:47], 1.0 op_sel_hi:[1,0]\n"
    "v_pk_add_f32 v[48:49], v[48:49], 1.0 op_sel_hi:[1,0]\n"
    "v_pk_add_f32 v[50:51], v[50:51], 1.0 op_sel_hi:[1,0]\n"
    "v_rcp_f32 v44, v44\n"
    "v_rcp_f32 v45, v45\n"
    "v_rcp_f32 v46, v46\n"
    "v_rcp_f32 v47, v47\n"
    "v_rcp_f32 v48, v48\n"
    "v_rcp_f32 v49, v49\n"
    "v_rcp_f32 v50, v50\n"
    "v_rcp_f32 v51, v51\n"
    "v_pk_mul_f32 v[36:37], v[36:37], v[44:45]\n"
    "v_pk_mul_f32 v[38:39], v[38:39], v[46:47]\n"
    "v_pk_mul_f32 v[40:41], v[40:41], v[48:49]\n"
    "v_pk_mul_f32 v[42:43], v[42:43], v[50:51]\n"
    "v_cvt_pk_bf16_f32 v44, v36, v37\n"
    "v_cvt_pk_bf16_f32 v45, v38, v39\n"
    "v_cvt_pk_bf16_f32 v46, v40, v41\n"
    "v_cvt_pk_bf16_f32 v47, v42, v43\n"
    "v_cndmask_b32 v76, v44, v46, s[62:63]\n"
    "v_cndmask_b32 v77, v45, v47, s[62:63]\n"
    "v_cndmask_b32 v78, v46, v44, s[62:63]\n"
    "v_cndmask_b32 v79, v47, v45, s[62:63]\n"
    "s_add_u32 s56, s64, 8192\n"
    "buffer_store_dwordx4 v[76:79], v241, s[36:39], s56 offen sc1\n"
    "v_pk_fma_f32 v[60:61], v[148:149], v[222:223], v[184:185] op_sel:[0,1,0] op_sel_hi:[1,1,1]\n"
    "v_pk_fma_f32 v[62:63], v[150:151], v[222:223], v[186:187] op_sel:[0,1,0] op_sel_hi:[1,1,1]\n"
    "v_pk_fma_f32 v[64:65], v[208:209], v[222:223], v[188:189] op_sel:[0,1,0] op_sel_hi:[1,1,1]\n"
    "v_pk_fma_f32 v[66:67], v[210:211], v[222:223], v[190:191] op_sel:[0,1,0] op_sel_hi:[1,1,1]\n"
    "v_pk_fma_f32 v[52:53], v[104:105], v[222:223], v[60:61] op_sel_hi:[1,0,1]\n"
    "v_pk_fma_f32 v[54:55], v[106:107], v[222:223], v[62:63] op_sel_hi:[1,0,1]\n"
    "v_pk_fma_f32 v[56:57], v[112:113], v[222:223], v[64:65] op_sel_hi:[1,0,1]\n"
    "v_pk_fma_f32 v[58:59], v[114:115], v[222:223], v[66:67] op_sel_hi:[1,0,1]\n"
    "v_pk_mul_f32 v[60:61], v[52:53], v[52:53]\n"
    "v_pk_mul_f32 v[62:63], v[54:55], v[54:55]\n"
    "v_pk_mul_f32 v[64:65], v[56:57], v[56:57]\n"
    "v_pk_mul_f32 v[66:67], v[58:59], v[58:59]\n"
    "v_pk_fma_f32 v[60:61], v[60:61], s[54:55], s[54:55] op_sel:[0,0,1] op_sel_hi:[1,0,1]\n"
    "v_pk_fma_f32 v[62:63], v[62:63], s[54:55], s[54:55] op_sel:[0,0,1] op_sel_hi:[1,0,1]\n"
    "v_pk_fma_f32 v[64:65], v[64:65], s[54:55], s[54:55] op_sel:[0,0,1] op_sel_hi:[1,0,1]\n"
    "v_pk_fma_f32 v[66:67], v[66:67], s[54:55], s[54:55] op_sel:[0,0,1] op_sel_hi:[1,0,1]\n"
    "v_pk_mul_f32 v[60:61], v[52:53], v[60:61]\n"
    "v_pk_mul_f32 v[62:63], v[54:55], v[62:63]\n"
    "v_pk_mul_f32 v[64:65], v[56:57], v[64:65]\n"
    "v_pk_mul_f32 v[66:67], v[58:59], v[66:67]\n"
    "v_exp_f32 v60, v60\n"
    "v_exp_f32 v61, v61\n"
    "v_exp_f32 v62, v62\n"
    "v_exp_f32 v63, v63\n"
    "v_exp_f32 v64, v64\n"
    "v_exp_f32 v65, v65\n"
    "v_exp_f32 v66, v66\n"
    "v_exp_f32 v67, v67\n"
    "v_pk_add_f32 v[60:61], v[60:61], 1.0 op_sel_hi:[1,0]\n"
    "v_pk_add_f32 v[62:63], v[62:63], 1.0 op_sel_hi:[1,0]\n"
    "v_pk_add_f32 v[64:65], v[64:65], 1.0 op_sel_hi:[1,0]\n"
    "v_pk_add_f32 v[66:67], v[66:67], 1.0 op_sel_hi:[1,0]\n"
    "v_rcp_f32 v60, v60\n"
    "v_rcp_f32 v61, v61\n"
    "v_rcp_f32 v62, v62\n"
    "v_rcp_f32 v63, v63\n"
    "v_rcp_f32 v64, v64\n"
    "v_rcp_f32 v65, v65\n"
    "v_rcp_f32 v66, v66\n"
    "v_rcp_f32 v67, v67\n"
    "v_pk_mul_f32 v[52:53], v[52:53], v[60:61]\n"
    "v_pk_mul_f32 v[54:55], v[54:55], v[62:63]\n"
    "v_pk_mul_f32 v[56:57], v[56:57], v[64:65]\n"
    "v_pk_mul_f32 v[58:59], v[58:59], v[66:67]\n"
    "v_cvt_pk_bf16_f32 v60, v52, v53\n"
    "v_cvt_pk_bf16_f32 v61, v54, v55\n"
    "v_cvt_pk_bf16_f32 v62, v56, v57\n"
    "v_cvt_pk_bf16_f32 v63, v58, v59\n"
    "v_cndmask_b32 v80, v60, v62, s[62:63]\n"
    "v_cndmask_b32 v81, v61, v63, s[62:63]\n"
    "v_cndmask_b32 v82, v62, v60, s[62:63]\n"
    "v_cndmask_b32 v83, v63, v61, s[62:63]\n"
    "s_add_u32 s57, s65, 0\n"
    "buffer_store_dwordx4 v[80:83], v241, s[36:39], s57 offen sc1\n"
    "v_pk_fma_f32 v[44:45], v[152:153], v[222:223], v[192:193] op_sel:[0,1,0] op_sel_hi:[1,1,1]\n"
    "v_pk_fma_f32 v[46:47], v[154:155], v[222:223], v[194:195] op_sel:[0,1,0] op_sel_hi:[1,1,1]\n"
    "v_pk_fma_f32 v[48:49], v[212:213], v[222:223], v[196:197] op_sel:[0,1,0] op_sel_hi:[1,1,1]\n"
    "v_pk_fma_f32 v[50:51], v[214:215], v[222:223], v[198:199] op_sel:[0,1,0] op_sel_hi:[1,1,1]\n"
    "v_pk_fma_f32 v[36:37], v[120:121], v[222:223], v[44:45] op_sel_hi:[1,0,1]\n"
    "v_pk_fma_f32 v[38:39], v[122:123], v[222:223], v[46:47] op_sel_hi:[1,0,1]\n"
    "v_pk_fma_f32 v[40:41], v[128:129], v[222:223], v[48:49] op_sel_hi:[1,0,1]\n"
    "v_pk_fma_f32 v[42:43], v[130:131], v[222:223], v[50:51] op_sel_hi:[1,0,1]\n"
    "v_pk_mul_f32 v[44:45], v[36:37], v[36:37]\n"
    "v_pk_mul_f32 v[46:47], v[38:39], v[38:39]\n"
    "v_pk_mul_f32 v[48:49], v[40:41], v[40:41]\n"
    "v_pk_mul_f32 v[50:51], v[42:43], v[42:43]\n"
    "v_pk_fma_f32 v[44:45], v[44:45], s[54:55], s[54:55] op_sel:[0,0,1] op_sel_hi:[1,0,1]\n"
    "v_pk_fma_f32 v[46:47], v[46:47], s[54:55], s[54:55] op_sel:[0,0,1] op_sel_hi:[1,0,1]\n"
    "v_pk_fma_f32 v[48:49], v[48:49], s[54:55], s[54:55] op_sel:[0,0,1] op_sel_hi:[1,0,1]\n"
    "v_pk_fma_f32 v[50:51], v[50:51], s[54:55], s[54:55] op_sel:[0,0,1] op_sel_hi:[1,0,1]\n"
    "v_pk_mul_f32 v[44:45], v[36:37], v[44:45]\n"
    "v_pk_mul_f32 v[46:47], v[38:39], v[46:47]\n"
    "v_pk_mul_f32 v[48:49], v[40:41], v[48:49]\n"
    "v_pk_mul_f32 v[50:51], v[42:43], v[50:51]\n"
    "v_exp_f32 v44, v44\n"
    "v_exp_f32 v45, v45\n"
    "v_exp_f32 v46, v46\n"
    "v_exp_f32 v47, v47\n"
    "v_exp_f32 v48, v48\n"
    "v_exp_f32 v49, v49\n"
    "v_exp_f32 v50, v50\n"
    "v_exp_f32 v51, v51\n"
    "v_pk_add_f32 v[44:45], v[44:45], 1.0 op_sel_hi:[1,0]\n"
    "v_pk_add_f32 v[46:47], v[46:47], 1.0 op_sel_hi:[1,0]\n"
    "v_pk_add_f32 v[48:49], v[48:49], 1.0 op_sel_hi:[1,0]\n"
    "v_pk_add_f32 v[50:51], v[50:51], 1.0 op_sel_hi:[1,0]\n"
    "v_rcp_f32 v44, v44\n"
    "v_rcp_f32 v45, v45\n"
    "v_rcp_f32 v46, v46\n"
    "v_rcp_f32 v47, v47\n"
    "v_rcp_f32 v48, v48\n"
    "v_rcp_f32 v49, v49\n"
    "v_rcp_f32 v50, v50\n"
    "v_rcp_f32 v51, v51\n"
    "v_pk_mul_f32 v[36:37], v[36:37], v[44:45]\n"
    "v_pk_mul_f32 v[38:39], v[38:39], v[46:47]\n"
    "v_pk_mul_f32 v[40:41], v[40:41], v[48:49]\n"
    "v_pk_mul_f32 v[42:43], v[42:43], v[50:51]\n"
    "v_cvt_pk_bf16_f32 v44, v36, v37\n"
    "v_cvt_pk_bf16_f32 v45, v38, v39\n"
    "v_cvt_pk_bf16_f32 v46, v40, v41\n"
    "v_cvt_pk_bf16_f32 v47, v42, v43\n"
    "v_cndmask_b32 v68, v44, v46, s[62:63]\n"
    "v_cndmask_b32 v69, v45, v47, s[62:63]\n"
    "v_cndmask_b32 v70, v46, v44, s[62:63]\n"
    "v_cndmask_b32 v71, v47, v45, s[62:63]\n"
    "s_add_u32 s56, s65, 4096\n"
    "buffer_store_dwordx4 v[68:71], v241, s[36:39], s56 offen sc1\n"
    "v_pk_fma_f32 v[60:61], v[156:157], v[222:223], v[200:201] op_sel:[0,1,0] op_sel_hi:[1,1,1]\n"
    "v_pk_fma_f32 v[62:63], v[158:159], v[222:223], v[202:203] op_sel:[0,1,0] op_sel_hi:[1,1,1]\n"
    "v_pk_fma_f32 v[64:65], v[216:217], v[222:223], v[204:205] op_sel:[0,1,0] op_sel_hi:[1,1,1]\n"
    "v_pk_fma_f32 v[66:67], v[218:219], v[222:223], v[206:207] op_sel:[0,1,0] op_sel_hi:[1,1,1]\n"
    "v_pk_fma_f32 v[52:53], v[136:137], v[222:223], v[60:61] op_sel_hi:[1,0,1]\n"
    "v_pk_fma_f32 v[54:55], v[138:139], v[222:223], v[62:63] op_sel_hi:[1,0,1]\n"
    "v_pk_fma_f32 v[56:57], v[144:145], v[222:223], v[64:65] op_sel_hi:[1,0,1]\n"
    "v_pk_fma_f32 v[58:59], v[146:147], v[222:223], v[66:67] op_sel_hi:[1,0,1]\n"
    "v_pk_mul_f32 v[60:61], v[52:53], v[52:53]\n"
    "v_pk_mul_f32 v[62:63], v[54:55], v[54:55]\n"
    "v_pk_mul_f32 v[64:65], v[56:57], v[56:57]\n"
    "v_pk_mul_f32 v[66:67], v[58:59], v[58:59]\n"
    "v_pk_fma_f32 v[60:61], v[60:61], s[54:55], s[54:55] op_sel:[0,0,1] op_sel_hi:[1,0,1]\n"
    "v_pk_fma_f32 v[62:63], v[62:63], s[54:55], s[54:55] op_sel:[0,0,1] op_sel_hi:[1,0,1]\n"
    "v_pk_fma_f32 v[64:65], v[64:65], s[54:55], s[54:55] op_sel:[0,0,1] op_sel_hi:[1,0,1]\n"
    "v_pk_fma_f32 v[66:67], v[66:67], s[54:55], s[54:55] op_sel:[0,0,1] op_sel_hi:[1,0,1]\n"
    "v_pk_mul_f32 v[60:61], v[52:53], v[60:61]\n"
    "v_pk_mul_f32 v[62:63], v[54:55], v[62:63]\n"
    "v_pk_mul_f32 v[64:65], v[56:57], v[64:65]\n"
    "v_pk_mul_f32 v[66:67], v[58:59], v[66:67]\n"
    "v_exp_f32 v60, v60\n"
    "v_exp_f32 v61, v61\n"
    "v_exp_f32 v62, v62\n"
    "v_exp_f32 v63, v63\n"
    "v_exp_f32 v64, v64\n"
    "v_exp_f32 v65, v65\n"
    "v_exp_f32 v66, v66\n"
    "v_exp_f32 v67, v67\n"
    "v_pk_add_f32 v[60:61], v[60:61], 1.0 op_sel_hi:[1,0]\n"
    "v_pk_add_f32 v[62:63], v[62:63], 1.0 op_sel_hi:[1,0]\n"
    "v_pk_add_f32 v[64:65], v[64:65], 1.0 op_sel_hi:[1,0]\n"
    "v_pk_add_f32 v[66:67], v[66:67], 1.0 op_sel_hi:[1,0]\n"
    "v_rcp_f32 v60, v60\n"
    "v_rcp_f32 v61, v61\n"
    "v_rcp_f32 v62, v62\n"
    "v_rcp_f32 v63, v63\n"
    "v_rcp_f32 v64, v64\n"
    "v_rcp_f32 v65, v65\n"
    "v_rcp_f32 v66, v66\n"
    "v_rcp_f32 v67, v67\n"
    "v_pk_mul_f32 v[52:53], v[52:53], v[60:61]\n"
    "v_pk_mul_f32 v[54:55], v[54:55], v[62:63]\n"
    "v_pk_mul_f32 v[56:57], v[56:57], v[64:65]\n"
    "v_pk_mul_f32 v[58:59], v[58:59], v[66:67]\n"
    "v_cvt_pk_bf16_f32 v60, v52, v53\n"
    "v_cvt_pk_bf16_f32 v61, v54, v55\n"
    "v_cvt_pk_bf16_f32 v62, v56, v57\n"
    "v_cvt_pk_bf16_f32 v63, v58, v59\n"
    "v_cndmask_b32 v72, v60, v62, s[62:63]\n"
    "v_cndmask_b32 v73, v61, v63, s[62:63]\n"
    "v_cndmask_b32 v74, v62, v60, s[62:63]\n"
    "v_cndmask_b32 v75, v63, v61, s[62:63]\n"
    "s_add_u32 s57, s65, 8192\n"
    "buffer_store_dwordx4 v[72:75], v241, s[36:39], s57 offen sc1\n"
    "s_add_u32 s58, s40, 0\n"
    "s_and_b32 s58, s58, 3\n"
    "s_lshl_b32 s58, s58, 10\n"
    "v_add_u32 v244, s58, v241\n"
    "s_add_u32 s58, s40, 1\n"
    "s_and_b32 s58, s58, 3\n"
    "s_lshl_b32 s58, s58, 10\n"
    "v_add_u32 v245, s58, v241\n"
    "s_add_u32 s58, s40, 2\n"
    "s_and_b32 s58, s58, 3\n"
    "s_lshl_b32 s58, s58, 10\n"
    "v_add_u32 v246, s58, v241\n"
    "s_add_u32 s58, s40, 3\n"
    "s_and_b32 s58, s58, 3\n"
    "s_lshl_b32 s58, s58, 10\n"
    "v_add_u32 v247, s58, v241\n"
    "v_and_b32 v243, 7, v240\n"
    "v_lshlrev_b32 v243, 3, v243\n"
    "v_mov_b32 v2, s52\n"
    "v_mov_b32 v3, s53\n"
    "s_lshl_b32 s58, s43, 8\n"
    "s_lshl_b32 s59, s44, 5\n"
    "s_add_u32 s58, s58, s59\n"
    "s_lshl_b32 s59, s40, 3\n"
    "s_add_u32 s58, s58, s59\n"
    "s_add_u32 s60, s58, 8192\n"
    "s_waitcnt vmcnt(0)\n"
    "s_mov_b64 exec, 1\n"
    "buffer_store_dwordx2 v[2:3], off, s[36:39], s60 sc1\n"
    "s_mov_b64 exec, -1\n"
    "ds_read_b64_tr_b16 a[40:41], v249 offset:32768\n"
    "ds_read_b64_tr_b16 a[42:43], v249 offset:35328\n"
    "ds_read_b64_tr_b16 a[44:45], v249 offset:32800\n"
    "ds_read_b64_tr_b16 a[46:47], v249 offset:35360\n"
    "ds_read_b64_tr_b16 a[48:49], v249 offset:32832\n"
    "ds_read_b64_tr_b16 a[50:51], v249 offset:35392\n"
    "ds_read_b64_tr_b16 a[52:53], v249 offset:32864\n"
    "ds_read_b64_tr_b16 a[54:55], v249 offset:35424\n"
    "ds_read_b64_tr_b16 a[56:57], v249 offset:32896\n"
    "ds_read_b64_tr_b16 a[58:59], v249 offset:35456\n"
    "s_waitcnt lgkmcnt(0)\n"
    "s_lshr_b64 s[8:9], s[92:93], 28\n"
    "s_and_b32 s8, s8, 15\n"
    "s_mul_i32 s8, s8, 40960\n"
    "s_add_u32 s11, s4, s8\n"
    "s_add_u32 m0, s7, 98304\n"
    "s_nop 0\n"
    "buffer_load_dwordx4 v251, s[24:27], s11 offen offset:0 lds\n"
    "buffer_load_dwordx4 v252, s[24:27], s11 offen offset:1024 lds\n"
    "buffer_load_dwordx4 v253, s[24:27], s11 offen offset:2048 lds\n"
    "buffer_load_dwordx4 v254, s[24:27], s11 offen offset:3072 lds\n"
    "s_add_u32 m0, s7, 102400\n"
    "s_nop 0\n"
    "buffer_load_dwordx4 v255, s[24:27], s11 offen offset:0 lds\n"
    "ds_read_b64_tr_b16 a[60:61], v248 offset:0\n"
    "ds_read_b64_tr_b16 a[62:63], v248 offset:2560\n"
    "ds_read_b64_tr_b16 a[64:65], v248 offset:32\n"
    "ds_read_b64_tr_b16 a[66:67], v248 offset:2592\n"
    "ds_read_b64_tr_b16 a[68:69], v248 offset:64\n"
    "ds_read_b64_tr_b16 a[70:71], v248 offset:2624\n"
    "ds_read_b64_tr_b16 a[72:73], v248 offset:96\n"
    "ds_read_b64_tr_b16 a[74:75], v248 offset:2656\n"
    "ds_read_b64_tr_b16 a[76:77], v248 offset:128\n"
    "ds_read_b64_tr_b16 a[78:79], v248 offset:2688\n"
    "s_waitcnt lgkmcnt(0)\n"
    "s_lshr_b64 s[8:9], s[92:93], 32\n"
    "s_and_b32 s8, s8, 15\n"
    "s_mul_i32 s8, s8, 40960\n"
    "s_add_u32 s11, s4, s8\n"
    "s_add_u32 m0, s7, 0\n"
    "s_nop 0\n"
    "buffer_load_dwordx4 v251, s[24:27], s11 offen offset:0 lds\n"
    "buffer_load_dwordx4 v252, s[24:27], s11 offen offset:1024 lds\n"
    "buffer_load_dwordx4 v253, s[24:27], s11 offen offset:2048 lds\n"
    "buffer_load_dwordx4 v254, s[24:27], s11 offen offset:3072 lds\n"
    "s_add_u32 m0, s7, 4096\n"
    "s_nop 0\n"
    "buffer_load_dwordx4 v255, s[24:27], s11 offen offset:0 lds\n"
    "ds_read_b64_tr_b16 a[80:81], v248 offset:32768\n"
    "ds_read_b64_tr_b16 a[82:83], v248 offset:35328\n"
    "ds_read_b64_tr_b16 a[84:85], v248 offset:32800\n"
    "ds_read_b64_tr_b16 a[86:87], v248 offset:35360\n"
    "ds_read_b64_tr_b16 a[88:89], v248 offset:32832\n"
    "ds_read_b64_tr_b16 a[90:91], v248 offset:35392\n"
    "ds_read_b64_tr_b16 a[92:93], v248 offset:32864\n"
    "ds_read_b64_tr_b16 a[94:95], v248 offset:35424\n"
    "ds_read_b64_tr_b16 a[96:97], v248 offset:32896\n"
    "ds_read_b64_tr_b16 a[98:99], v248 offset:35456\n"
    "s_waitcnt lgkmcnt(0)\n"
    "s_lshr_b64 s[8:9], s[92:93], 36\n"
    "s_and_b32 s8, s8, 15\n"
    "s_mul_i32 s8, s8, 40960\n"
    "s_add_u32 s11, s4, s8\n"
    "s_add_u32 m0, s7, 32768\n"
    "s_nop 0\n"
    "buffer_load_dwordx4 v251, s[24:27], s11 offen offset:0 lds\n"
    "buffer_load_dwordx4 v252, s[24:27], s11 offen offset:1024 lds\n"
    "buffer_load_dwordx4 v253, s[24:27], s11 offen offset:2048 lds\n"
    "buffer_load_dwordx4 v254, s[24:27], s11 offen offset:3072 lds\n"
    "s_add_u32 m0, s7, 36864\n"
    "s_nop 0\n"
    "buffer_load_dwordx4 v255, s[24:27], s11 offen offset:0 lds\n"
    "ds_read_b64_tr_b16 a[100:101], v250 offset:0\n"
    "ds_read_b64_tr_b16 a[102:103], v250 offset:2560\n"
    "ds_read_b64_tr_b16 a[104:105], v250 offset:32\n"
    "ds_read_b64_tr_b16 a[106:107], v250 offset:2592\n"
    "ds_read_b64_tr_b16 a[108:109], v250 offset:64\n"
    "ds_read_b64_tr_b16 a[110:111], v250 offset:2624\n"
    "ds_read_b64_tr_b16 a[112:113], v250 offset:96\n"
    "ds_read_b64_tr_b16 a[114:115], v250 offset:2656\n"
    "ds_read_b64_tr_b16 a[116:117], v250 offset:128\n"
    "ds_read_b64_tr_b16 a[118:119], v250 offset:2688\n"
    "s_waitcnt lgkmcnt(0)\n"
    "s_lshr_b64 s[8:9], s[92:93], 40\n"
    "s_and_b32 s8, s8, 15\n"
    "s_mul_i32 s8, s8, 40960\n"
    "s_add_u32 s11, s4, s8\n"
    "s_add_u32 m0, s7, 131072\n"
    "s_nop 0\n"
    "buffer_load_dwordx4 v251, s[24:27], s11 offen offset:0 lds\n"
    "buffer_load_dwordx4 v252, s[24:27], s11 offen offset:1024 lds\n"
    "buffer_load_dwordx4 v253, s[24:27], s11 offen offset:2048 lds\n"
    "buffer_load_dwordx4 v254, s[24:27], s11 offen offset:3072 lds\n"
    "s_add_u32 m0, s7, 135168\n"
    "s_nop 0\n"
    "buffer_load_dwordx4 v255, s[24:27], s11 offen offset:0 lds\n"
    "ds_read_b64_tr_b16 a[120:121], v249 offset:0\n"
    "ds_read_b64_tr_b16 a[122:123], v249 offset:2560\n"
    "ds_read_b64_tr_b16 a[124:125], v249 offset:32\n"
    "ds_read_b64_tr_b16 a[126:127], v249 offset:2592\n"
    "ds_read_b64_tr_b16 a[128:129], v249 offset:64\n"
    "ds_read_b64_tr_b16 a[130:131], v249 offset:2624\n"
    "ds_read_b64_tr_b16 a[132:133], v249 offset:96\n"
    "ds_read_b64_tr_b16 a[134:135], v249 offset:2656\n"
    "ds_read_b64_tr_b16 a[136:137], v249 offset:128\n"
    "ds_read_b64_tr_b16 a[138:139], v249 offset:2688\n"
    "s_waitcnt lgkmcnt(0)\n"
    "s_lshr_b64 s[8:9], s[92:93], 44\n"
    "s_and_b32 s8, s8, 15\n"
    "s_mul_i32 s8, s8, 40960\n"
    "s_add_u32 s11, s4, s8\n"
    "s_add_u32 m0, s7, 65536\n"
    "s_nop 0\n"
    "buffer_load_dwordx4 v251, s[24:27], s11 offen offset:0 lds\n"
    "buffer_load_dwordx4 v252, s[24:27], s11 offen offset:1024 lds\n"
    "buffer_load_dwordx4 v253, s[24:27], s11 offen offset:2048 lds\n"
    "buffer_load_dwordx4 v254, s[24:27], s11 offen offset:3072 lds\n"
    "s_add_u32 m0, s7, 69632\n"
    "s_nop 0\n"
    "buffer_load_dwordx4 v255, s[24:27], s11 offen offset:0 lds\n"
    "s_waitcnt vmcnt(20)\n"
    "ds_read_b64_tr_b16 a[140:141], v249 offset:32768\n"
    "ds_read_b64_tr_b16 a[142:143], v249 offset:35328\n"
    "ds_read_b64_tr_b16 a[144:145], v249 offset:32800\n"
    "ds_read_b64_tr_b16 a[146:147], v249 offset:35360\n"
    "ds_read_b64_tr_b16 a[148:149], v249 offset:32832\n"
    "ds_read_b64_tr_b16 a[150:151], v249 offset:35392\n"
    "ds_read_b64_tr_b16 a[152:153], v249 offset:32864\n"
    "ds_read_b64_tr_b16 a[154:155], v249 offset:35424\n"
    "ds_read_b64_tr_b16 a[156:157], v249 offset:32896\n"
    "ds_read_b64_tr_b16 a[158:159], v249 offset:35456\n"
    "s_waitcnt vmcnt(15)\n"
    "ds_read_b64_tr_b16 a[160:161], v248 offset:0\n"
    "ds_read_b64_tr_b16 a[162:163], v248 offset:2560\n"
    "ds_read_b64_tr_b16 a[164:165], v248 offset:32\n"
    "ds_read_b64_tr_b16 a[166:167], v248 offset:2592\n"
    "ds_read_b64_tr_b16 a[168:169], v248 offset:64\n"
    "ds_read_b64_tr_b16 a[170:171], v248 offset:2624\n"
    "ds_read_b64_tr_b16 a[172:173], v248 offset:96\n"
    "ds_read_b64_tr_b16 a[174:175], v248 offset:2656\n"
    "ds_read_b64_tr_b16 a[176:177], v248 offset:128\n"
    "ds_read_b64_tr_b16 a[178:179], v248 offset:2688\n"
    "s_waitcnt vmcnt(10)\n"
    "ds_read_b64_tr_b16 a[180:181], v248 offset:32768\n"
    "ds_read_b64_tr_b16 a[182:183], v248 offset:35328\n"
    "ds_read_b64_tr_b16 a[184:185], v248 offset:32800\n"
    "ds_read_b64_tr_b16 a[186:187], v248 offset:35360\n"
    "ds_read_b64_tr_b16 a[188:189], v248 offset:32832\n"
    "ds_read_b64_tr_b16 a[190:191], v248 offset:35392\n"
    "ds_read_b64_tr_b16 a[192:193], v248 offset:32864\n"
    "ds_read_b64_tr_b16 a[194:195], v248 offset:35424\n"
    "ds_read_b64_tr_b16 a[196:197], v248 offset:32896\n"
    "ds_read_b64_tr_b16 a[198:199], v248 offset:35456\n"
    "buffer_load_dwordx2 v[236:237], v243, s[36:39], s99 offen sc1\n"
    "s_waitcnt vmcnt(6)\n"
    "ds_read_b64_tr_b16 a[200:201], v250 offset:0\n"
    "ds_read_b64_tr_b16 a[202:203], v250 offset:2560\n"
    "ds_read_b64_tr_b16 a[204:205], v250 offset:32\n"
    "ds_read_b64_tr_b16 a[206:207], v250 offset:2592\n"
    "ds_read_b64_tr_b16 a[208:209], v250 offset:64\n"
    "ds_read_b64_tr_b16 a[210:211], v250 offset:2624\n"
    "ds_read_b64_tr_b16 a[212:213], v250 offset:96\n"
    "ds_read_b64_tr_b16 a[214:215], v250 offset:2656\n"
    "ds_read_b64_tr_b16 a[216:217], v250 offset:128\n"
    "ds_read_b64_tr_b16 a[218:219], v250 offset:2688\n"
    "s_waitcnt vmcnt(1)\n"
    "ds_read_b64_tr_b16 a[220:221], v249 offset:0\n"
    "ds_read_b64_tr_b16 a[222:223], v249 offset:2560\n"
    "ds_read_b64_tr_b16 a[224:225], v249 offset:32\n"
    "ds_read_b64_tr_b16 a[226:227], v249 offset:2592\n"
    "ds_read_b64_tr_b16 a[228:229], v249 offset:64\n"
    "ds_read_b64_tr_b16 a[230:231], v249 offset:2624\n"
    "ds_read_b64_tr_b16 a[232:233], v249 offset:96\n"
    "ds_read_b64_tr_b16 a[234:235], v249 offset:2656\n"
    "ds_read_b64_tr_b16 a[236:237], v249 offset:128\n"
    "ds_read_b64_tr_b16 a[238:239], v249 offset:2688\n"
    "s_waitcnt vmcnt(0)\n"
    "v_cmp_eq_u64 vcc, s[52:53], v[236:237]\n"
    "s_nop 0\n"
    "s_cmp_eq_u64 vcc, exec\n"
    "s_cbranch_scc1 Lfhok_8%=\n"
    "s_mov_b32 s98, 0\n"
    "Lfhlp_9%=:\n"
    "buffer_load_dwordx2 v[236:237], v243, s[36:39], s99 offen sc1\n"
    "s_waitcnt vmcnt(0)\n"
    "v_cmp_eq_u64 vcc, s[52:53], v[236:237]\n"
    "s_add_u32 s98, s98, 1\n"
    "s_cmp_eq_u64 vcc, exec\n"
    "s_cbranch_scc1 Lfhok_8%=\n"
    "s_cmp_lt_u32 s98, 262144\n"
    "s_cbranch_scc1 Lfhlp_9%=\n"
    "Lfhok_8%=:\n"
    "s_lshr_b64 s[8:9], s[92:93], 0\n"
    "s_and_b32 s8, s8, 15\n"
    "s_mul_i32 s8, s8, 4096\n"
    "s_add_u32 s97, s3, s8\n"
    "buffer_load_dwordx4 v[80:83], v244, s[36:39], s97 offen sc1\n"
    "buffer_load_dwordx4 v[84:87], v245, s[36:39], s97 offen sc1\n"
    "buffer_load_dwordx4 v[88:91], v246, s[36:39], s97 offen sc1\n"
    "buffer_load_dwordx4 v[92:95], v247, s[36:39], s97 offen sc1\n"
    "s_lshr_b64 s[8:9], s[92:93], 4\n"
    "s_and_b32 s8, s8, 15\n"
    "s_mul_i32 s8, s8, 4096\n"
    "s_add_u32 s97, s3, s8\n"
    "buffer_load_dwordx4 v[96:99], v244, s[36:39], s97 offen sc1\n"
    "buffer_load_dwordx4 v[100:103], v245, s[36:39], s97 offen sc1\n"
    "buffer_load_dwordx4 v[104:107], v246, s[36:39], s97 offen sc1\n"
    "buffer_load_dwordx4 v[108:111], v247, s[36:39], s97 offen sc1\n"
    "s_lshr_b64 s[8:9], s[92:93], 8\n"
    "s_and_b32 s8, s8, 15\n"
    "s_mul_i32 s8, s8, 4096\n"
    "s_add_u32 s97, s3, s8\n"
    "buffer_load_dwordx4 v[112:115], v244, s[36:39], s97 offen sc1\n"
    "buffer_load_dwordx4 v[116:119], v245, s[36:39], s97 offen sc1\n"
    "buffer_load_dwordx4 v[120:123], v246, s[36:39], s97 offen sc1\n"
    "buffer_load_dwordx4 v[124:127], v247, s[36:39], s97 offen sc1\n"
    "s_lshr_b64 s[8:9], s[92:93], 12\n"
    "s_and_b32 s8, s8, 15\n"
    "s_mul_i32 s8, s8, 4096\n"
    "s_add_u32 s97, s3, s8\n"
    "buffer_load_dwordx4 v[128:131], v244, s[36:39], s97 offen sc1\n"
    "buffer_load_dwordx4 v[132:135], v245, s[36:39], s97 offen sc1\n"
    "buffer_load_dwordx4 v[136:139], v246, s[36:39], s97 offen sc1\n"
    "buffer_load_dwordx4 v[140:143], v247, s[36:39], s97 offen sc1\n"
    "s_waitcnt lgkmcnt(0)\n"
    "s_waitcnt vmcnt(15)\n"
    "v_mfma_f32_16x16x32_bf16 v[0:3], a[0:3], v[80:83], 0\n"
    "v_mfma_f32_16x16x32_bf16 v[4:7], a[4:7], v[80:83], 0\n"
    "v_mfma_f32_16x16x32_bf16 v[8:11], a[8:11], v[80:83], 0\n"
    "s_lshr_b64 s[8:9], s[92:93], 16\n"
    "v_mfma_f32_16x16x32_bf16 v[12:15], a[12:15], v[80:83], 0\n"
    "v_mfma_f32_16x16x32_bf16 v[16:19], a[16:19], v[80:83], 0\n"
    "s_and_b32 s8, s8, 15\n"
    "s_waitcnt vmcnt(14)\n"
    "v_mfma_f32_16x16x32_bf16 v[20:23], a[0:3], v[84:87], 0\n"
    "v_mfma_f32_16x16x32_bf16 v[24:27], a[4:7], v[84:87], 0\n"
    "v_mfma_f32_16x16x32_bf16 v[28:31], a[8:11], v[84:87], 0\n"
    "s_mul_i32 s8, s8, 4096\n"
    "v_mfma_f32_16x16x32_bf16 v[32:35], a[12:15], v[84:87], 0\n"
    "v_mfma_f32_16x16x32_bf16 v[36:39], a[16:19], v[84:87], 0\n"
    "s_add_u32 s97, s3, s8\n"
    "s_waitcnt vmcnt(13)\n"
    "v_mfma_f32_16x16x32_bf16 v[40:43], a[0:3], v[88:91], 0\n"
    "v_mfma_f32_16x16x32_bf16 v[44:47], a[4:7], v[88:91], 0\n"
    "v_mfma_f32_16x16x32_bf16 v[48:51], a[8:11], v[88:91], 0\n"
    "buffer_load_dwordx4 v[144:147], v244, s[36:39], s97 offen sc1\n"
    "v_mfma_f32_16x16x32_bf16 v[52:55], a[12:15], v[88:91], 0\n"
    "v_mfma_f32_16x16x32_bf16 v[56:59], a[16:19], v[88:91], 0\n"
    "buffer_load_dwordx4 v[148:151], v245, s[36:39], s97 offen sc1\n"
    "s_waitcnt vmcnt(14)\n"
    "v_mfma_f32_16x16x32_bf16 v[60:63], a[0:3], v[92:95], 0\n"
    "v_mfma_f32_16x16x32_bf16 v[64:67], a[4:7], v[92:95], 0\n"
    "v_mfma_f32_16x16x32_bf16 v[68:71], a[8:11], v[92:95], 0\n"
    "buffer_load_dwordx4 v[152:155], v246, s[36:39], s97 offen sc1\n"
    "v_mfma_f32_16x16x32_bf16 v[72:75], a[12:15], v[92:95], 0\n"
    "v_mfma_f32_16x16x32_bf16 v[76:79], a[16:19], v[92:95], 0\n"
    "buffer_load_dwordx4 v[156:159], v247, s[36:39], s97 offen sc1\n"
    "s_waitcnt vmcnt(15)\n"
    "v_mfma_f32_16x16x32_bf16 v[0:3], a[20:23], v[96:99], v[0:3]\n"
    "v_mfma_f32_16x16x32_bf16 v[4:7], a[24:27], v[96:99], v[4:7]\n"
    "v_mfma_f32_16x16x32_bf16 v[8:11], a[28:31], v[96:99], v[8:11]\n"
    "s_lshr_b64 s[8:9], s[92:93], 20\n"
    "v_mfma_f32_16x16x32_bf16 v[12:15], a[32:35], v[96:99], v[12:15]\n"
    "v_mfma_f32_16x16x32_bf16 v[16:19], a[36:39], v[96:99], v[16:19]\n"
    "s_and_b32 s8, s8, 15\n"
    "s_waitcnt vmcnt(14)\n"
    "v_mfma_f32_16x16x32_bf16 v[20:23], a[20:23], v[100:103], v[20:23]\n"
    "v_mfma_f32_16x16x32_bf16 v[24:27], a[24:27], v[100:103], v[24:27]\n"
    "v_mfma_f32_16x16x32_bf16 v[28:31], a[28:31], v[100:103], v[28:31]\n"
    "s_mul_i32 s8, s8, 4096\n"
    "v_mfma_f32_16x16x32_bf16 v[32:35], a[32:35], v[100:103], v[32:35]\n"
    "v_mfma_f32_16x16x32_bf16 v[36:39], a[36:39], v[100:103], v[36:39]\n"
    "s_add_u32 s97, s3, s8\n"
    "s_waitcnt vmcnt(13)\n"
    "v_mfma_f32_16x16x32_bf16 v[40:43], a[20:23], v[104:107], v[40:43]\n"
    "v_mfma_f32_16x16x32_bf16 v[44:47], a[24:27], v[104:107], v[44:47]\n"
    "v_mfma_f32_16x16x32_bf16 v[48:51], a[28:31], v[104:107], v[48:51]\n"
    "buffer_load_dwordx4 v[160:163], v244, s[36:39], s97 offen sc1\n"
    "v_mfma_f32_16x16x32_bf16 v[52:55], a[32:35], v[104:107], v[52:55]\n"
    "v_mfma_f32_16x16x32_bf16 v[56:59], a[36:39], v[104:107], v[56:59]\n"
    "buffer_load_dwordx4 v[164:167], v245, s[36:39], s97 offen sc1\n"
    "s_waitcnt vmcnt(14)\n"
    "v_mfma_f32_16x16x32_bf16 v[60:63], a[20:23], v[108:111], v[60:63]\n"
    "v_mfma_f32_16x16x32_bf16 v[64:67], a[24:27], v[108:111], v[64:67]\n"
    "v_mfma_f32_16x16x32_bf16 v[68:71], a[28:31], v[108:111], v[68:71]\n"
    "buffer_load_dwordx4 v[168:171], v246, s[36:39], s97 offen sc1\n"
    "v_mfma_f32_16x16x32_bf16 v[72:75], a[32:35], v[108:111], v[72:75]\n"
    "v_mfma_f32_16x16x32_bf16 v[76:79], a[36:39], v[108:111], v[76:79]\n"
    "buffer_load_dwordx4 v[172:175], v247, s[36:39], s97 offen sc1\n"
    "s_waitcnt vmcnt(15)\n"
    "v_mfma_f32_16x16x32_bf16 v[0:3], a[40:43], v[112:115], v[0:3]\n"
    "v_mfma_f32_16x16x32_bf16 v[4:7], a[44:47], v[112:115], v[4:7]\n"
    "v_mfma_f32_16x16x32_bf16 v[8:11], a[48:51], v[112:115], v[8:11]\n"
    "s_lshr_b64 s[8:9], s[92:93], 24\n"
    "v_mfma_f32_16x16x32_bf16 v[12:15], a[52:55], v[112:115], v[12:15]\n"
    "v_mfma_f32_16x16x32_bf16 v[16:19], a[56:59], v[112:115], v[16:19]\n"
    "s_and_b32 s8, s8, 15\n"
    "s_waitcnt vmcnt(14)\n"
    "v_mfma_f32_16x16x32_bf16 v[20:23], a[40:43], v[116:119], v[20:23]\n"
    "v_mfma_f32_16x16x32_bf16 v[24:27], a[44:47], v[116:119], v[24:27]\n"
    "v_mfma_f32_16x16x32_bf16 v[28:31], a[48:51], v[116:119], v[28:31]\n"
    "s_mul_i32 s8, s8, 4096\n"
    "v_mfma_f32_16x16x32_bf16 v[32:35], a[52:55], v[116:119], v[32:35]\n"
    "v_mfma_f32_16x16x32_bf16 v[36:39], a[56:59], v[116:119], v[36:39]\n"
    "s_add_u32 s97, s3, s8\n"
    "s_waitcnt vmcnt(13)\n"
    "v_mfma_f32_16x16x32_bf16 v[40:43], a[40:43], v[120:123], v[40:43]\n"
    "v_mfma_f32_16x16x32_bf16 v[44:47], a[44:47], v[120:123], v[44:47]\n"
    "v_mfma_f32_16x16x32_bf16 v[48:51], a[48:51], v[120:123], v[48:51]\n"
    "buffer_load_dwordx4 v[176:179], v244, s[36:39], s97 offen sc1\n"
    "v_mfma_f32_16x16x32_bf16 v[52:55], a[52:55], v[120:123], v[52:55]\n"
    "v_mfma_f32_16x16x32_bf16 v[56:59], a[56:59], v[120:123], v[56:59]\n"
    "buffer_load_dwordx4 v[180:183], v245, s[36:39], s97 offen sc1\n"
    "s_waitcnt vmcnt(14)\n"
    "v_mfma_f32_16x16x32_bf16 v[60:63], a[40:43], v[124:127], v[60:63]\n"
    "v_mfma_f32_16x16x32_bf16 v[64:67], a[44:47], v[124:127], v[64:67]\n"
    "v_mfma_f32_16x16x32_bf16 v[68:71], a[48:51], v[124:127], v[68:71]\n"
    "buffer_load_dwordx4 v[184:187], v246, s[36:39], s97 offen sc1\n"
    "v_mfma_f32_16x16x32_bf16 v[72:75], a[52:55], v[124:127], v[72:75]\n"
    "v_mfma_f32_16x16x32_bf16 v[76:79], a[56:59], v[124:127], v[76:79]\n"
    "buffer_load_dwordx4 v[188:191], v247, s[36:39], s97 offen sc1\n"
    "s_waitcnt vmcnt(15)\n"
    "v_mfma_f32_16x16x32_bf16 v[0:3], a[60:63], v[128:131], v[0:3]\n"
    "v_mfma_f32_16x16x32_bf16 v[4:7], a[64:67], v[128:131], v[4:7]\n"
    "v_mfma_f32_16x16x32_bf16 v[8:11], a[68:71], v[128:131], v[8:11]\n"
    "s_lshr_b64 s[8:9], s[92:93], 28\n"
    "v_mfma_f32_16x16x32_bf16 v[12:15], a[72:75], v[128:131], v[12:15]\n"
    "v_mfma_f32_16x16x32_bf16 v[16:19], a[76:79], v[128:131], v[16:19]\n"
    "s_and_b32 s8, s8, 15\n"
    "s_waitcnt vmcnt(14)\n"
    "v_mfma_f32_16x16x32_bf16 v[20:23], a[60:63], v[132:135], v[20:23]\n"
    "v_mfma_f32_16x16x32_bf16 v[24:27], a[64:67], v[132:135], v[24:27]\n"
    "v_mfma_f32_16x16x32_bf16 v[28:31], a[68:71], v[132:135], v[28:31]\n"
    "s_mul_i32 s8, s8, 4096\n"
    "v_mfma_f32_16x16x32_bf16 v[32:35], a[72:75], v[132:135], v[32:35]\n"
    "v_mfma_f32_16x16x32_bf16 v[36:39], a[76:79], v[132:135], v[36:39]\n"
    "s_add_u32 s97, s3, s8\n"
    "s_waitcnt vmcnt(13)\n"
    "v_mfma_f32_16x16x32_bf16 v[40:43], a[60:63], v[136:139], v[40:43]\n"
    "v_mfma_f32_16x16x32_bf16 v[44:47], a[64:67], v[136:139], v[44:47]\n"
    "v_mfma_f32_16x16x32_bf16 v[48:51], a[68:71], v[136:139], v[48:51]\n"
    "buffer_load_dwordx4 v[192:195], v244, s[36:39], s97 offen sc1\n"
    "v_mfma_f32_16x16x32_bf16 v[52:55], a[72:75], v[136:139], v[52:55]\n"
    "v_mfma_f32_16x16x32_bf16 v[56:59], a[76:79], v[136:139], v[56:59]\n"
    "buffer_load_dwordx4 v[196:199], v245, s[36:39], s97 offen sc1\n"
    "s_waitcnt vmcnt(14)\n"
    "v_mfma_f32_16x16x32_bf16 v[60:63], a[60:63], v[140:143], v[60:63]\n"
    "v_mfma_f32_16x16x32_bf16 v[64:67], a[64:67], v[140:143], v[64:67]\n"
    "v_mfma_f32_16x16x32_bf16 v[68:71], a[68:71], v[140:143], v[68:71]\n"
    "buffer_load_dwordx4 v[200:203], v246, s[36:39], s97 offen sc1\n"
    "v_mfma_f32_16x16x32_bf16 v[72:75], a[72:75], v[140:143], v[72:75]\n"
    "v_mfma_f32_16x16x32_bf16 v[76:79], a[76:79], v[140:143], v[76:79]\n"
    "buffer_load_dwordx4 v[204:207], v247, s[36:39], s97 offen sc1\n"
    "s_waitcnt vmcnt(15)\n"
    "v_mfma_f32_16x16x32_bf16 v[0:3], a[80:83], v[144:147], v[0:3]\n"
    "v_mfma_f32_16x16x32_bf16 v[4:7], a[84:87], v[144:147], v[4:7]\n"
    "v_mfma_f32_16x16x32_bf16 v[8:11], a[88:91], v[144:147], v[8:11]\n"
    "s_lshr_b64 s[8:9], s[92:93], 32\n"
    "v_mfma_f32_16x16x32_bf16 v[12:15], a[92:95], v[144:147], v[12:15]\n"
    "v_mfma_f32_16x16x32_bf16 v[16:19], a[96:99], v[144:147], v[16:19]\n"
    "s_and_b32 s8, s8, 15\n"
    "s_waitcnt vmcnt(14)\n"
    "v_mfma_f32_16x16x32_bf16 v[20:23], a[80:83], v[148:151], v[20:23]\n"
    "v_mfma_f32_16x16x32_bf16 v[24:27], a[84:87], v[148:151], v[24:27]\n"
    "v_mfma_f32_16x16x32_bf16 v[28:31], a[88:91], v[148:151], v[28:31]\n"
    "s_mul_i32 s8, s8, 4096\n"
    "v_mfma_f32_16x16x32_bf16 v[32:35], a[92:95], v[148:151], v[32:35]\n"
    "v_mfma_f32_16x16x32_bf16 v[36:39], a[96:99], v[148:151], v[36:39]\n"
    "s_add_u32 s97, s3, s8\n"
    "s_waitcnt vmcnt(13)\n"
    "v_mfma_f32_16x16x32_bf16 v[40:43], a[80:83], v[152:155], v[40:43]\n"
    "v_mfma_f32_16x16x32_bf16 v[44:47], a[84:87], v[152:155], v[44:47]\n"
    "v_mfma_f32_16x16x32_bf16 v[48:51], a[88:91], v[152:155], v[48:51]\n"
    "buffer_load_dwordx4 v[80:83], v244, s[36:39], s97 offen sc1\n"
    "v_mfma_f32_16x16x32_bf16 v[52:55], a[92:95], v[152:155], v[52:55]\n"
    "v_mfma_f32_16x16x32_bf16 v[56:59], a[96:99], v[152:155], v[56:59]\n"
    "buffer_load_dwordx4 v[84:87], v245, s[36:39], s97 offen sc1\n"
    "s_waitcnt vmcnt(14)\n"
    "v_mfma_f32_16x16x32_bf16 v[60:63], a[80:83], v[156:159], v[60:63]\n"
    "v_mfma_f32_16x16x32_bf16 v[64:67], a[84:87], v[156:159], v[64:67]\n"
    "v_mfma_f32_16x16x32_bf16 v[68:71], a[88:91], v[156:159], v[68:71]\n"
    "buffer_load_dwordx4 v[88:91], v246, s[36:39], s97 offen sc1\n"
    "v_mfma_f32_16x16x32_bf16 v[72:75], a[92:95], v[156:159], v[72:75]\n"
    "v_mfma_f32_16x16x32_bf16 v[76:79], a[96:99], v[156:159], v[76:79]\n"
    "buffer_load_dwordx4 v[92:95], v247, s[36:39], s97 offen sc1\n"
    "s_waitcnt vmcnt(15)\n"
    "v_mfma_f32_16x16x32_bf16 v[0:3], a[100:103], v[160:163], v[0:3]\n"
    "v_mfma_f32_16x16x32_bf16 v[4:7], a[104:107], v[160:163], v[4:7]\n"
    "v_mfma_f32_16x16x32_bf16 v[8:11], a[108:111], v[160:163], v[8:11]\n"
    "s_lshr_b64 s[8:9], s[92:93], 36\n"
    "v_mfma_f32_16x16x32_bf16 v[12:15], a[112:115], v[160:163], v[12:15]\n"
    "v_mfma_f32_16x16x32_bf16 v[16:19], a[116:119], v[160:163], v[16:19]\n"
    "s_and_b32 s8, s8, 15\n"
    "s_waitcnt vmcnt(14)\n"
    "v_mfma_f32_16x16x32_bf16 v[20:23], a[100:103], v[164:167], v[20:23]\n"
    "v_mfma_f32_16x16x32_bf16 v[24:27], a[104:107], v[164:167], v[24:27]\n"
    "v_mfma_f32_16x16x32_bf16 v[28:31], a[108:111], v[164:167], v[28:31]\n"
    "s_mul_i32 s8, s8, 4096\n"
    "v_mfma_f32_16x16x32_bf16 v[32:35], a[112:115], v[164:167], v[32:35]\n"
    "v_mfma_f32_16x16x32_bf16 v[36:39], a[116:119], v[164:167], v[36:39]\n"
    "s_add_u32 s97, s3, s8\n"
    "s_waitcnt vmcnt(13)\n"
    "v_mfma_f32_16x16x32_bf16 v[40:43], a[100:103], v[168:171], v[40:43]\n"
    "v_mfma_f32_16x16x32_bf16 v[44:47], a[104:107], v[168:171], v[44:47]\n"
    "v_mfma_f32_16x16x32_bf16 v[48:51], a[108:111], v[168:171], v[48:51]\n"
    "buffer_load_dwordx4 v[96:99], v244, s[36:39], s97 offen sc1\n"
    "v_mfma_f32_16x16x32_bf16 v[52:55], a[112:115], v[168:171], v[52:55]\n"
    "v_mfma_f32_16x16x32_bf16 v[56:59], a[116:119], v[168:171], v[56:59]\n"
    "buffer_load_dwordx4 v[100:103], v245, s[36:39], s97 offen sc1\n"
    "s_waitcnt vmcnt(14)\n"
    "v_mfma_f32_16x16x32_bf16 v[60:63], a[100:103], v[172:175], v[60:63]\n"
    "v_mfma_f32_16x16x32_bf16 v[64:67], a[104:107], v[172:175], v[64:67]\n"
    "v_mfma_f32_16x16x32_bf16 v[68:71], a[108:111], v[172:175], v[68:71]\n"
    "buffer_load_dwordx4 v[104:107], v246, s[36:39], s97 offen sc1\n"
    "v_mfma_f32_16x16x32_bf16 v[72:75], a[112:115], v[172:175], v[72:75]\n"
    "v_mfma_f32_16x16x32_bf16 v[76:79], a[116:119], v[172:175], v[76:79]\n"
    "buffer_load_dwordx4 v[108:111], v247, s[36:39], s97 offen sc1\n"
    "s_waitcnt vmcnt(15)\n"
    "v_mfma_f32_16x16x32_bf16 v[0:3], a[120:123], v[176:179], v[0:3]\n"
    "v_mfma_f32_16x16x32_bf16 v[4:7], a[124:127], v[176:179], v[4:7]\n"
    "v_mfma_f32_16x16x32_bf16 v[8:11], a[128:131], v[176:179], v[8:11]\n"
    "s_lshr_b64 s[8:9], s[92:93], 40\n"
    "v_mfma_f32_16x16x32_bf16 v[12:15], a[132:135], v[176:179], v[12:15]\n"
    "v_mfma_f32_16x16x32_bf16 v[16:19], a[136:139], v[176:179], v[16:19]\n"
    "s_and_b32 s8, s8, 15\n"
    "s_waitcnt vmcnt(14)\n"
    "v_mfma_f32_16x16x32_bf16 v[20:23], a[120:123], v[180:183], v[20:23]\n"
    "v_mfma_f32_16x16x32_bf16 v[24:27], a[124:127], v[180:183], v[24:27]\n"
    "v_mfma_f32_16x16x32_bf16 v[28:31], a[128:131], v[180:183], v[28:31]\n"
    "s_mul_i32 s8, s8, 4096\n"
    "v_mfma_f32_16x16x32_bf16 v[32:35], a[132:135], v[180:183], v[32:35]\n"
    "v_mfma_f32_16x16x32_bf16 v[36:39], a[136:139], v[180:183], v[36:39]\n"
    "s_add_u32 s97, s3, s8\n"
    "s_waitcnt vmcnt(13)\n"
    "v_mfma_f32_16x16x32_bf16 v[40:43], a[120:123], v[184:187], v[40:43]\n"
    "v_mfma_f32_16x16x32_bf16 v[44:47], a[124:127], v[184:187], v[44:47]\n"
    "v_mfma_f32_16x16x32_bf16 v[48:51], a[128:131], v[184:187], v[48:51]\n"
    "buffer_load_dwordx4 v[112:115], v244, s[36:39], s97 offen sc1\n"
    "v_mfma_f32_16x16x32_bf16 v[52:55], a[132:135], v[184:187], v[52:55]\n"
    "v_mfma_f32_16x16x32_bf16 v[56:59], a[136:139], v[184:187], v[56:59]\n"
    "buffer_load_dwordx4 v[116:119], v245, s[36:39], s97 offen sc1\n"
    "s_waitcnt vmcnt(14)\n"
    "v_mfma_f32_16x16x32_bf16 v[60:63], a[120:123], v[188:191], v[60:63]\n"
    "v_mfma_f32_16x16x32_bf16 v[64:67], a[124:127], v[188:191], v[64:67]\n"
    "v_mfma_f32_16x16x32_bf16 v[68:71], a[128:131], v[188:191], v[68:71]\n"
    "buffer_load_dwordx4 v[120:123], v246, s[36:39], s97 offen sc1\n"
    "v_mfma_f32_16x16x32_bf16 v[72:75], a[132:135], v[188:191], v[72:75]\n"
    "v_mfma_f32_16x16x32_bf16 v[76:79], a[136:139], v[188:191], v[76:79]\n"
    "buffer_load_dwordx4 v[124:127], v247, s[36:39], s97 offen sc1\n"
    "s_waitcnt vmcnt(15)\n"
    "v_mfma_f32_16x16x32_bf16 v[0:3], a[140:143], v[192:195], v[0:3]\n"
    "v_mfma_f32_16x16x32_bf16 v[4:7], a[144:147], v[192:195], v[4:7]\n"
    "v_mfma_f32_16x16x32_bf16 v[8:11], a[148:151], v[192:195], v[8:11]\n"
    "s_lshr_b64 s[8:9], s[92:93], 44\n"
    "v_mfma_f32_16x16x32_bf16 v[12:15], a[152:155], v[192:195], v[12:15]\n"
    "v_mfma_f32_16x16x32_bf16 v[16:19], a[156:159], v[192:195], v[16:19]\n"
    "s_and_b32 s8, s8, 15\n"
    "s_waitcnt vmcnt(14)\n"
    "v_mfma_f32_16x16x32_bf16 v[20:23], a[140:143], v[196:199], v[20:23]\n"
    "v_mfma_f32_16x16x32_bf16 v[24:27], a[144:147], v[196:199], v[24:27]\n"
    "v_mfma_f32_16x16x32_bf16 v[28:31], a[148:151], v[196:199], v[28:31]\n"
    "s_mul_i32 s8, s8, 4096\n"
    "v_mfma_f32_16x16x32_bf16 v[32:35], a[152:155], v[196:199], v[32:35]\n"
    "v_mfma_f32_16x16x32_bf16 v[36:39], a[156:159], v[196:199], v[36:39]\n"
    "s_add_u32 s97, s3, s8\n"
    "s_waitcnt vmcnt(13)\n"
    "v_mfma_f32_16x16x32_bf16 v[40:43], a[140:143], v[200:203], v[40:43]\n"
    "v_mfma_f32_16x16x32_bf16 v[44:47], a[144:147], v[200:203], v[44:47]\n"
    "v_mfma_f32_16x16x32_bf16 v[48:51], a[148:151], v[200:203], v[48:51]\n"
    "buffer_load_dwordx4 v[128:131], v244, s[36:39], s97 offen sc1\n"
    "v_mfma_f32_16x16x32_bf16 v[52:55], a[152:155], v[200:203], v[52:55]\n"
    "v_mfma_f32_16x16x32_bf16 v[56:59], a[156:159], v[200:203], v[56:59]\n"
    "buffer_load_dwordx4 v[132:135], v245, s[36:39], s97 offen sc1\n"
    "s_waitcnt vmcnt(14)\n"
    "v_mfma_f32_16x16x32_bf16 v[60:63], a[140:143], v[204:207], v[60:63]\n"
    "v_mfma_f32_16x16x32_bf16 v[64:67], a[144:147], v[204:207], v[64:67]\n"
    "v_mfma_f32_16x16x32_bf16 v[68:71], a[148:151], v[204:207], v[68:71]\n"
    "buffer_load_dwordx4 v[136:139], v246, s[36:39], s97 offen sc1\n"
    "v_mfma_f32_16x16x32_bf16 v[72:75], a[152:155], v[204:207], v[72:75]\n"
    "v_mfma_f32_16x16x32_bf16 v[76:79], a[156:159], v[204:207], v[76:79]\n"
    "buffer_load_dwordx4 v[140:143], v247, s[36:39], s97 offen sc1\n"
    "s_waitcnt vmcnt(15)\n"
    "v_mfma_f32_16x16x32_bf16 v[0:3], a[160:163], v[80:83], v[0:3]\n"
    "v_mfma_f32_16x16x32_bf16 v[4:7], a[164:167], v[80:83], v[4:7]\n"
    "v_mfma_f32_16x16x32_bf16 v[8:11], a[168:171], v[80:83], v[8:11]\n"
    "v_lshrrev_b32 v230, 4, v240\n"
    "v_mfma_f32_16x16x32_bf16 v[12:15], a[172:175], v[80:83], v[12:15]\n"
    "v_mfma_f32_16x16x32_bf16 v[16:19], a[176:179], v[80:83], v[16:19]\n"
    "v_lshlrev_b32 v230, 3, v230\n"
    "s_waitcnt vmcnt(14)\n"
    "v_mfma_f32_16x16x32_bf16 v[20:23], a[160:163], v[84:87], v[20:23]\n"
    "v_mfma_f32_16x16x32_bf16 v[24:27], a[164:167], v[84:87], v[24:27]\n"
    "v_mfma_f32_16x16x32_bf16 v[28:31], a[168:171], v[84:87], v[28:31]\n"
    "s_mul_i32 s58, s46, 160\n"
    "v_mfma_f32_16x16x32_bf16 v[32:35], a[172:175], v[84:87], v[32:35]\n"
    "v_mfma_f32_16x16x32_bf16 v[36:39], a[176:179], v[84:87], v[36:39]\n"
    "buffer_load_dwordx2 v[220:221], v230, s[28:31], s58 offen offset:0\n"
    "s_waitcnt vmcnt(14)\n"
    "v_mfma_f32_16x16x32_bf16 v[40:43], a[160:163], v[88:91], v[40:43]\n"
    "v_mfma_f32_16x16x32_bf16 v[44:47], a[164:167], v[88:91], v[44:47]\n"
    "v_mfma_f32_16x16x32_bf16 v[48:51], a[168:171], v[88:91], v[48:51]\n"
    "buffer_load_dwordx2 v[222:223], v230, s[28:31], s58 offen offset:32\n"
    "v_mfma_f32_16x16x32_bf16 v[52:55], a[172:175], v[88:91], v[52:55]\n"
    "v_mfma_f32_16x16x32_bf16 v[56:59], a[176:179], v[88:91], v[56:59]\n"
    "buffer_load_dwordx2 v[224:225], v230, s[28:31], s58 offen offset:64\n"
    "s_waitcnt vmcnt(15)\n"
    "v_mfma_f32_16x16x32_bf16 v[60:63], a[160:163], v[92:95], v[60:63]\n"
    "v_mfma_f32_16x16x32_bf16 v[64:67], a[164:167], v[92:95], v[64:67]\n"
    "v_mfma_f32_16x16x32_bf16 v[68:71], a[168:171], v[92:95], v[68:71]\n"
    "buffer_load_dwordx2 v[226:227], v230, s[28:31], s58 offen offset:96\n"
    "v_mfma_f32_16x16x32_bf16 v[72:75], a[172:175], v[92:95], v[72:75]\n"
    "v_mfma_f32_16x16x32_bf16 v[76:79], a[176:179], v[92:95], v[76:79]\n"
    "buffer_load_dwordx2 v[228:229], v230, s[28:31], s58 offen offset:128\n"
    "s_cmp_lg_u32 s10, 0\n"
    "s_cbranch_scc1 Lgl_10%=\n"
    "s_cmp_lg_u32 s40, 0\n"
    "s_cbranch_scc1 Lgl_10%=\n"
    "v_lshlrev_b32 v243, 7, v240\n"
    "s_mov_b32 s61, 8192\n"
    "buffer_load_dwordx2 v[210:211], v243, s[36:39], s61 offen offset:0 sc1\n"
    "buffer_load_dwordx2 v[212:213], v243, s[36:39], s61 offen offset:32 sc1\n"
    "buffer_load_dwordx2 v[214:215], v243, s[36:39], s61 offen offset:64 sc1\n"
    "buffer_load_dwordx2 v[216:217], v243, s[36:39], s61 offen offset:96 sc1\n"
    "Lgl_10%=:\n"
    "s_waitcnt vmcnt(16)\n"
    "v_mfma_f32_16x16x32_bf16 v[0:3], a[180:183], v[96:99], v[0:3]\n"
    "v_mfma_f32_16x16x32_bf16 v[4:7], a[184:187], v[96:99], v[4:7]\n"
    "s_add_u32 s58, s40, 1\n"
    "v_mfma_f32_16x16x32_bf16 v[8:11], a[188:191], v[96:99], v[8:11]\n"
    "s_and_b32 s58, s58, 3\n"
    "v_mfma_f32_16x16x32_bf16 v[12:15], a[192:195], v[96:99], v[12:15]\n"
    "s_mul_i32 s58, s58, 15360\n"
    "v_mfma_f32_16x16x32_bf16 v[16:19], a[196:199], v[96:99], v[16:19]\n"
    "s_add_u32 s58, s58, 0\n"
    "s_waitcnt vmcnt(15)\n"
    "v_mfma_f32_16x16x32_bf16 v[20:23], a[180:183], v[100:103], v[20:23]\n"
    "v_add_u32 v231, s58, v241\n"
    "v_mfma_f32_16x16x32_bf16 v[24:27], a[184:187], v[100:103], v[24:27]\n"
    "v_mfma_f32_16x16x32_bf16 v[28:31], a[188:191], v[100:103], v[28:31]\n"
    "s_add_u32 s58, s40, 2\n"
    "v_mfma_f32_16x16x32_bf16 v[32:35], a[192:195], v[100:103], v[32:35]\n"
    "s_and_b32 s58, s58, 3\n"
    "v_mfma_f32_16x16x32_bf16 v[36:39], a[196:199], v[100:103], v[36:39]\n"
    "s_mul_i32 s58, s58, 15360\n"
    "s_waitcnt vmcnt(14)\n"
    "v_mfma_f32_16x16x32_bf16 v[40:43], a[180:183], v[104:107], v[40:43]\n"
    "s_add_u32 s58, s58, 5120\n"
    "v_mfma_f32_16x16x32_bf16 v[44:47], a[184:187], v[104:107], v[44:47]\n"
    "v_add_u32 v232, s58, v241\n"
    "v_mfma_f32_16x16x32_bf16 v[48:51], a[188:191], v[104:107], v[48:51]\n"
    "s_add_u32 s58, s40, 3\n"
    "v_mfma_f32_16x16x32_bf16 v[52:55], a[192:195], v[104:107], v[52:55]\n"
    "v_mfma_f32_16x16x32_bf16 v[56:59], a[196:199], v[104:107], v[56:59]\n"
    "s_and_b32 s58, s58, 3\n"
    "s_waitcnt vmcnt(13)\n"
    "v_mfma_f32_16x16x32_bf16 v[60:63], a[180:183], v[108:111], v[60:63]\n"
    "s_mul_i32 s58, s58, 15360\n"
    "v_mfma_f32_16x16x32_bf16 v[64:67], a[184:187], v[108:111], v[64:67]\n"
    "s_add_u32 s58, s58, 10240\n"
    "v_mfma_f32_16x16x32_bf16 v[68:71], a[188:191], v[108:111], v[68:71]\n"
    "v_add_u32 v233, s58, v241\n"
    "v_mfma_f32_16x16x32_bf16 v[72:75], a[192:195], v[108:111], v[72:75]\n"
    "s_mul_i32 s58, s40, 15360\n"
    "v_mfma_f32_16x16x32_bf16 v[76:79], a[196:199], v[108:111], v[76:79]\n"
    "v_add_u32 v234, s58, v241\n"
    "s_waitcnt vmcnt(12)\n"
    "v_mfma_f32_16x16x32_bf16 v[0:3], a[200:203], v[112:115], v[0:3]\n"
    "v_mfma_f32_16x16x32_bf16 v[4:7], a[204:207], v[112:115], v[4:7]\n"
    "v_mfma_f32_16x16x32_bf16 v[8:11], a[208:211], v[112:115], v[8:11]\n"
    "v_and_b32 v235, 15, v240\n"
    "v_mfma_f32_16x16x32_bf16 v[12:15], a[212:215], v[112:115], v[12:15]\n"
    "v_mfma_f32_16x16x32_bf16 v[16:19], a[216:219], v[112:115], v[16:19]\n"
    "v_mul_u32_u24 v235, 1280, v235\n"
    "s_waitcnt vmcnt(11)\n"
    "v_mfma_f32_16x16x32_bf16 v[20:23], a[200:203], v[116:119], v[20:23]\n"
    "v_mfma_f32_16x16x32_bf16 v[24:27], a[204:207], v[116:119], v[24:27]\n"
    "v_mfma_f32_16x16x32_bf16 v[28:31], a[208:211], v[116:119], v[28:31]\n"
    "v_add_u32 v235, v235, v230\n"
    "v_mfma_f32_16x16x32_bf16 v[32:35], a[212:215], v[116:119], v[32:35]\n"
    "v_mfma_f32_16x16x32_bf16 v[36:39], a[216:219], v[116:119], v[36:39]\n"
    "s_lshl_b32 s58, s45, 2\n"
    "s_waitcnt vmcnt(10)\n"
    "v_mfma_f32_16x16x32_bf16 v[40:43], a[200:203], v[120:123], v[40:43]\n"
    "v_mfma_f32_16x16x32_bf16 v[44:47], a[204:207], v[120:123], v[44:47]\n"
    "v_mfma_f32_16x16x32_bf16 v[48:51], a[208:211], v[120:123], v[48:51]\n"
    "s_add_u32 s58, s58, s40\n"
    "v_mfma_f32_16x16x32_bf16 v[52:55], a[212:215], v[120:123], v[52:55]\n"
    "v_mfma_f32_16x16x32_bf16 v[56:59], a[216:219], v[120:123], v[56:59]\n"
    "s_mul_i32 s58, s58, 20480\n"
    "s_waitcnt vmcnt(9)\n"
    "v_mfma_f32_16x16x32_bf16 v[60:63], a[200:203], v[124:127], v[60:63]\n"
    "v_mfma_f32_16x16x32_bf16 v[64:67], a[204:207], v[124:127], v[64:67]\n"
    "v_mfma_f32_16x16x32_bf16 v[68:71], a[208:211], v[124:127], v[68:71]\n"
    "s_mul_i32 s59, s46, 160\n"
    "v_mfma_f32_16x16x32_bf16 v[72:75], a[212:215], v[124:127], v[72:75]\n"
    "v_mfma_f32_16x16x32_bf16 v[76:79], a[216:219], v[124:127], v[76:79]\n"
    "s_add_u32 s60, s58, s59\n"
    "s_waitcnt vmcnt(7)\n"
    "v_mfma_f32_16x16x32_bf16 v[20:23], a[220:223], v[132:135], v[20:23]\n"
    "v_mfma_f32_16x16x32_bf16 v[24:27], a[224:227], v[132:135], v[24:27]\n"
    "v_mfma_f32_16x16x32_bf16 v[28:31], a[228:231], v[132:135], v[28:31]\n"
    "v_mfma_f32_16x16x32_bf16 v[32:35], a[232:235], v[132:135], v[32:35]\n"
    "v_mfma_f32_16x16x32_bf16 v[36:39], a[236:239], v[132:135], v[36:39]\n"
    "s_waitcnt vmcnt(6)\n"
    "v_mfma_f32_16x16x32_bf16 v[40:43], a[220:223], v[136:139], v[40:43]\n"
    "s_nop 1\n"
    "ds_write_b128 v231, v[20:23] offset:0\n"
    "v_mfma_f32_16x16x32_bf16 v[44:47], a[224:227], v[136:139], v[44:47]\n"
    "ds_write_b128 v231, v[24:27] offset:1024\n"
    "v_mfma_f32_16x16x32_bf16 v[48:51], a[228:231], v[136:139], v[48:51]\n"
    "ds_write_b128 v231, v[28:31] offset:2048\n"
    "v_mfma_f32_16x16x32_bf16 v[52:55], a[232:235], v[136:139], v[52:55]\n"
    "ds_write_b128 v231, v[32:35] offset:3072\n"
    "v_mfma_f32_16x16x32_bf16 v[56:59], a[236:239], v[136:139], v[56:59]\n"
    "ds_write_b128 v231, v[36:39] offset:4096\n"
    "s_waitcnt vmcnt(5)\n"
    "v_mfma_f32_16x16x32_bf16 v[60:63], a[220:223], v[140:143], v[60:63]\n"
    "ds_write_b128 v232, v[40:43] offset:0\n"
    "v_mfma_f32_16x16x32_bf16 v[64:67], a[224:227], v[140:143], v[64:67]\n"
    "ds_write_b128 v232, v[44:47] offset:1024\n"
    "v_mfma_f32_16x16x32_bf16 v[68:71], a[228:231], v[140:143], v[68:71]\n"
    "ds_write_b128 v232, v[48:51] offset:2048\n"
    "v_mfma_f32_16x16x32_bf16 v[72:75], a[232:235], v[140:143], v[72:75]\n"
    "ds_write_b128 v232, v[52:55] offset:3072\n"
    "v_mfma_f32_16x16x32_bf16 v[76:79], a[236:239], v[140:143], v[76:79]\n"
    "ds_write_b128 v232, v[56:59] offset:4096\n"
    "v_mfma_f32_16x16x32_bf16 v[0:3], a[220:223], v[128:131], v[0:3]\n"
    "ds_write_b128 v233, v[60:63] offset:0\n"
    "v_mfma_f32_16x16x32_bf16 v[4:7], a[224:227], v[128:131], v[4:7]\n"
    "ds_write_b128 v233, v[64:67] offset:1024\n"
    "v_mfma_f32_16x16x32_bf16 v[8:11], a[228:231], v[128:131], v[8:11]\n"
    "ds_write_b128 v233, v[68:71] offset:2048\n"
    "v_mfma_f32_16x16x32_bf16 v[12:15], a[232:235], v[128:131], v[12:15]\n"
    "ds_write_b128 v233, v[72:75] offset:3072\n"
    "v_mfma_f32_16x16x32_bf16 v[16:19], a[236:239], v[128:131], v[16:19]\n"
    "ds_write_b128 v233, v[76:79] offset:4096\n"
    "s_waitcnt vmcnt(0)\n"
    "v_lshlrev_b32 v180, 16, v220\n"
    "v_and_b32 v181, 0xffff0000, v220\n"
    "v_lshlrev_b32 v182, 16, v221\n"
    "v_and_b32 v183, 0xffff0000, v221\n"
    "v_lshlrev_b32 v184, 16, v222\n"
    "v_and_b32 v185, 0xffff0000, v222\n"
    "v_lshlrev_b32 v186, 16, v223\n"
    "v_and_b32 v187, 0xffff0000, v223\n"
    "v_lshlrev_b32 v188, 16, v224\n"
    "v_and_b32 v189, 0xffff0000, v224\n"
    "v_lshlrev_b32 v190, 16, v225\n"
    "v_and_b32 v191, 0xffff0000, v225\n"
    "v_lshlrev_b32 v192, 16, v226\n"
    "v_and_b32 v193, 0xffff0000, v226\n"
    "v_lshlrev_b32 v194, 16, v227\n"
    "v_and_b32 v195, 0xffff0000, v227\n"
    "v_lshlrev_b32 v196, 16, v228\n"
    "v_and_b32 v197, 0xffff0000, v228\n"
    "v_lshlrev_b32 v198, 16, v229\n"
    "v_and_b32 v199, 0xffff0000, v229\n"
    "v_pk_add_f32 v[0:1], v[0:1], v[180:181]\n"
    "v_pk_add_f32 v[2:3], v[2:3], v[182:183]\n"
    "v_pk_add_f32 v[4:5], v[4:5], v[184:185]\n"
    "v_pk_add_f32 v[6:7], v[6:7], v[186:187]\n"
    "v_pk_add_f32 v[8:9], v[8:9], v[188:189]\n"
    "v_pk_add_f32 v[10:11], v[10:11], v[190:191]\n"
    "v_pk_add_f32 v[12:13], v[12:13], v[192:193]\n"
    "v_pk_add_f32 v[14:15], v[14:15], v[194:195]\n"
    "v_pk_add_f32 v[16:17], v[16:17], v[196:197]\n"
    "v_pk_add_f32 v[18:19], v[18:19], v[198:199]\n"
    "s_waitcnt lgkmcnt(0)\n"
    "s_barrier\n"
    "ds_read_b128 v[80:83], v234 offset:0\n"
    "ds_read_b128 v[100:103], v234 offset:5120\n"
    "ds_read_b128 v[120:123], v234 offset:10240\n"
    "ds_read_b128 v[84:87], v234 offset:1024\n"
    "ds_read_b128 v[104:107], v234 offset:6144\n"
    "ds_read_b128 v[124:127], v234 offset:11264\n"
    "ds_read_b128 v[88:91], v234 offset:2048\n"
    "ds_read_b128 v[108:111], v234 offset:7168\n"
    "ds_read_b128 v[128:131], v234 offset:12288\n"
    "ds_read_b128 v[92:95], v234 offset:3072\n"
    "ds_read_b128 v[112:115], v234 offset:8192\n"
    "ds_read_b128 v[132:135], v234 offset:13312\n"
    "ds_read_b128 v[96:99], v234 offset:4096\n"
    "ds_read_b128 v[116:119], v234 offset:9216\n"
    "ds_read_b128 v[136:139], v234 offset:14336\n"
    "s_waitcnt lgkmcnt(12)\n"
    "v_pk_add_f32 v[80:81], v[80:81], v[100:101]\n"
    "v_pk_add_f32 v[82:83], v[82:83], v[102:103]\n"
    "v_pk_add_f32 v[0:1], v[0:1], v[120:121]\n"
    "v_pk_add_f32 v[2:3], v[2:3], v[122:123]\n"
    "v_pk_add_f32 v[0:1], v[0:1], v[80:81]\n"
    "v_pk_add_f32 v[2:3], v[2:3], v[82:83]\n"
    "v_cvt_pk_bf16_f32 v224, v0, v1\n"
    "v_cvt_pk_bf16_f32 v225, v2, v3\n"
    "buffer_store_dwordx2 v[224:225], v235, s[32:35], s60 offen offset:0\n"
    "s_waitcnt lgkmcnt(9)\n"
    "v_pk_add_f32 v[84:85], v[84:85], v[104:105]\n"
    "v_pk_add_f32 v[86:87], v[86:87], v[106:107]\n"
    "v_pk_add_f32 v[4:5], v[4:5], v[124:125]\n"
    "v_pk_add_f32 v[6:7], v[6:7], v[126:127]\n"
    "v_pk_add_f32 v[4:5], v[4:5], v[84:85]\n"
    "v_pk_add_f32 v[6:7], v[6:7], v[86:87]\n"
    "v_cvt_pk_bf16_f32 v226, v4, v5\n"
    "v_cvt_pk_bf16_f32 v227, v6, v7\n"
    "buffer_store_dwordx2 v[226:227], v235, s[32:35], s60 offen offset:32\n"
    "s_waitcnt lgkmcnt(6)\n"
    "v_pk_add_f32 v[88:89], v[88:89], v[108:109]\n"
    "v_pk_add_f32 v[90:91], v[90:91], v[110:111]\n"
    "v_pk_add_f32 v[8:9], v[8:9], v[128:129]\n"
    "v_pk_add_f32 v[10:11], v[10:11], v[130:131]\n"
    "v_pk_add_f32 v[8:9], v[8:9], v[88:89]\n"
    "v_pk_add_f32 v[10:11], v[10:11], v[90:91]\n"
    "v_cvt_pk_bf16_f32 v228, v8, v9\n"
    "v_cvt_pk_bf16_f32 v229, v10, v11\n"
    "buffer_store_dwordx2 v[228:229], v235, s[32:35], s60 offen offset:64\n"
    "s_waitcnt lgkmcnt(3)\n"
    "v_pk_add_f32 v[92:93], v[92:93], v[112:113]\n"
    "v_pk_add_f32 v[94:95], v[94:95], v[114:115]\n"
    "v_pk_add_f32 v[12:13], v[12:13], v[132:133]\n"
    "v_pk_add_f32 v[14:15], v[14:15], v[134:135]\n"
    "v_pk_add_f32 v[12:13], v[12:13], v[92:93]\n"
    "v_pk_add_f32 v[14:15], v[14:15], v[94:95]\n"
    "v_cvt_pk_bf16_f32 v224, v12, v13\n"
    "v_cvt_pk_bf16_f32 v225, v14, v15\n"
    "buffer_store_dwordx2 v[224:225], v235, s[32:35], s60 offen offset:96\n"
    "s_waitcnt lgkmcnt(0)\n"
    "v_pk_add_f32 v[96:97], v[96:97], v[116:117]\n"
    "v_pk_add_f32 v[98:99], v[98:99], v[118:119]\n"
    "v_pk_add_f32 v[16:17], v[16:17], v[136:137]\n"
    "v_pk_add_f32 v[18:19], v[18:19], v[138:139]\n"
    "v_pk_add_f32 v[16:17], v[16:17], v[96:97]\n"
    "v_pk_add_f32 v[18:19], v[18:19], v[98:99]\n"
    "v_cvt_pk_bf16_f32 v226, v16, v17\n"
    "v_cvt_pk_bf16_f32 v227, v18, v19\n"
    "buffer_store_dwordx2 v[226:227], v235, s[32:35], s60 offen offset:128\n"
    "s_cmp_lg_u32 s10, 0\n"
    "s_cbranch_scc1 Lgu_11%=\n"
    "s_cmp_lg_u32 s40, 0\n"
    "s_cbranch_scc1 Lgu_11%=\n"
    "s_mov_b32 s98, 0\n"
    "Lgulp_12%=:\n"
    "s_waitcnt vmcnt(0)\n"
    "v_cmp_eq_u64 vcc, s[52:53], v[210:211]\n"
    "v_cmp_eq_u64 s[56:57], s[52:53], v[212:213]\n"
    "s_nop 0\n"
    "s_and_b64 vcc, vcc, s[56:57]\n"
    "v_cmp_eq_u64 s[56:57], s[52:53], v[214:215]\n"
    "s_nop 0\n"
    "s_and_b64 vcc, vcc, s[56:57]\n"
    "v_cmp_eq_u64 s[56:57], s[52:53], v[216:217]\n"
    "s_nop 0\n"
    "s_and_b64 vcc, vcc, s[56:57]\n"
    "s_cmp_eq_u64 vcc, exec\n"
    "s_cbranch_scc1 Lguok_13%=\n"
    "buffer_load_dwordx2 v[210:211], v243, s[36:39], s61 offen offset:0 sc1\n"
    "buffer_load_dwordx2 v[212:213], v243, s[36:39], s61 offen offset:32 sc1\n"
    "buffer_load_dwordx2 v[214:215], v243, s[36:39], s61 offen offset:64 sc1\n"
    "buffer_load_dwordx2 v[216:217], v243, s[36:39], s61 offen offset:96 sc1\n"
    "s_add_u32 s98, s98, 1\n"
    "s_cmp_lt_u32 s98, 262144\n"
    "s_cbranch_scc1 Lgulp_12%=\n"
    "Lguok_13%=:\n"
    "v_mov_b32 v236, s52\n"
    "v_mov_b32 v237, s53\n"
    "s_mov_b64 exec, 1\n"
    "buffer_store_dwordx2 v[236:237], off, s[36:39], 0 sc1\n"
    "s_mov_b64 exec, -1\n"
    "Lgu_11%=:\n"
    "s_branch Lend_14%=\n"
    ".p2align 4\n"
    "Ltab_3%=:\n"
    ".quad 0x9876543210\n"
    ".quad 0xba9876543210\n"
    ".quad 0x9876543210\n"
    ".quad 0xba9876543210\n"
    ".quad 0x9876543210\n"
    ".quad 0xba9876543210\n"
    ".quad 0x9876543210\n"
    ".quad 0xba9876543210\n"
    ".quad 0x8947650321\n"
    ".quad 0x8ba947650321\n"
    ".quad 0x7983654021\n"
    ".quad 0xb7a983654021\n"
    ".quad 0x6987254301\n"
    ".quad 0xab6987254301\n"
    ".quad 0x9587614320\n"
    ".quad 0x9ba587614320\n"
    ".quad 0x9854761032\n"
    ".quad 0x98ba54761032\n"
    ".quad 0x8794365102\n"
    ".quad 0xb87a94365102\n"
    ".quad 0x7698325410\n"
    ".quad 0xba7698325410\n"
    ".quad 0x9658721430\n"
    ".quad 0xa9b658721430\n"
    ".quad 0x8965472103\n"
    ".quad 0xa98b65472103\n"
    ".quad 0x9875436210\n"
    ".quad 0xb987a5436210\n"
    ".quad 0x8769432501\n"
    ".quad 0xab8769432501\n"
    ".quad 0x9765832140\n"
    ".quad 0xba9765832140\n"
    "Lend_14%=:\n"
    "Lcend_1%=:\n"
    :: "s"(__builtin_amdgcn_kernarg_segment_ptr()), "s"(blockIdx.x), "v"(threadIdx.x)
    : "v1", "v2", "v3", "v4", "v5", "v6", "v7", "v8", "v9", "v10", "v11", "v12", "v13", "v14", "v15", "v16", "v17", "v18", "v19", "v20", "v21", "v22", "v23", "v24", "v25", "v26", "v27", "v28", "v29", "v30", "v31", "v32", "v33", "v34", "v35", "v36", "v37", "v38", "v39", "v40", "v41", "v42", "v43", "v44", "v45", "v46", "v47", "v48", "v49", "v50", "v51", "v52", "v53", "v54", "v55", "v56", "v57", "v58", "v59", "v60", "v61", "v62", "v63", "v64", "v65", "v66", "v67", "v68", "v69", "v70", "v71", "v72", "v73", "v74", "v75", "v76", "v77", "v78", "v79", "v80", "v81", "v82", "v83", "v84", "v85", "v86", "v87", "v88", "v89", "v90", "v91", "v92", "v93", "v94", "v95", "v96", "v97", "v98", "v99", "v100", "v101", "v102", "v103", "v104", "v105", "v106", "v107", "v108", "v109", "v110", "v111", "v112", "v113", "v114", "v115", "v116", "v117", "v118", "v119", "v120", "v121", "v122", "v123", "v124", "v125", "v126", "v127", "v128", "v129", "v130", "v131", "v132", "v133", "v134", "v135", "v136", "v137", "v138", "v139", "v140", "v141", "v142", "v143", "v144", "v145", "v146", "v147", "v148", "v149", "v150", "v151", "v152", "v153", "v154", "v155", "v156", "v157", "v158", "v159", "v160", "v161", "v162", "v163", "v164", "v165", "v166", "v167", "v168", "v169", "v170", "v171", "v172", "v173", "v174", "v175", "v176", "v177", "v178", "v179", "v180", "v181", "v182", "v183", "v184", "v185", "v186", "v187", "v188", "v189", "v190", "v191", "v192", "v193", "v194", "v195", "v196", "v197", "v198", "v199", "v200", "v201", "v202", "v203", "v204", "v205", "v206", "v207", "v208", "v209", "v210", "v211", "v212", "v213", "v214", "v215", "v216", "v217", "v218", "v219", "v220", "v221", "v222", "v223", "v224", "v225", "v226", "v227", "v228", "v229", "v230", "v231", "v232", "v233", "v234", "v235", "v236", "v237", "v238", "v239", "v240", "v241", "v242", "v243", "v244", "v245", "v246", "v247", "v248", "v249", "v250", "v251", "v252", "v253", "v254", "v255", "a0", "a1", "a2", "a3", "a4", "a5", "a6", "a7", "a8", "a9", "a10", "a11", "a12", "a13", "a14", "a15", "a16", "a17", "a18", "a19", "a20", "a21", "a22", "a23", "a24", "a25", "a26", "a27", "a28", "a29", "a30", "a31", "a32", "a33", "a34", "a35", "a36", "a37", "a38", "a39", "a40", "a41", "a42", "a43", "a44", "a45", "a46", "a47", "a48", "a49", "a50", "a51", "a52", "a53", "a54", "a55", "a56", "a57", "a58", "a59", "a60", "a61", "a62", "a63", "a64", "a65", "a66", "a67", "a68", "a69", "a70", "a71", "a72", "a73", "a74", "a75", "a76", "a77", "a78", "a79", "a80", "a81", "a82", "a83", "a84", "a85", "a86", "a87", "a88", "a89", "a90", "a91", "a92", "a93", "a94", "a95", "a96", "a97", "a98", "a99", "a100", "a101", "a102", "a103", "a104", "a105", "a106", "a107", "a108", "a109", "a110", "a111", "a112", "a113", "a114", "a115", "a116", "a117", "a118", "a119", "a120", "a121", "a122", "a123", "a124", "a125", "a126", "a127", "a128", "a129", "a130", "a131", "a132", "a133", "a134", "a135", "a136", "a137", "a138", "a139", "a140", "a141", "a142", "a143", "a144", "a145", "a146", "a147", "a148", "a149", "a150", "a151", "a152", "a153", "a154", "a155", "a156", "a157", "a158", "a159", "a160", "a161", "a162", "a163", "a164", "a165", "a166", "a167", "a168", "a169", "a170", "a171", "a172", "a173", "a174", "a175", "a176", "a177", "a178", "a179", "a180", "a181", "a182", "a183", "a184", "a185", "a186", "a187", "a188", "a189", "a190", "a191", "a192", "a193", "a194", "a195", "a196", "a197", "a198", "a199", "a200", "a201", "a202", "a203", "a204", "a205", "a206", "a207", "a208", "a209", "a210", "a211", "a212", "a213", "a214", "a215", "a216", "a217", "a218", "a219", "a220", "a221", "a222", "a223", "a224", "a225", "a226", "a227", "a228", "a229", "a230", "a231", "a232", "a233", "a234", "a235", "a236", "a237", "a238", "a239", "a240", "a241", "a242", "a243", "a244", "a245", "a246", "a247", "a248", "a249", "a250", "a251", "a252", "a253", "a254", "a255", "s3", "s4", "s5", "s6", "s7", "s8", "s9", "s10", "s11", "s12", "s13", "s14", "s15", "s16", "s17", "s18", "s19", "s20", "s21", "s22", "s23", "s24", "s25", "s26", "s27", "s28", "s29", "s30", "s31", "s32", "s33", "s34", "s35", "s36", "s37", "s38", "s39", "s40", "s41", "s42", "s43", "s44", "s45", "s46", "s47", "s48", "s49", "s50", "s51", "s52", "s53", "s54", "s55", "s56", "s57", "s58", "s59", "s60", "s61", "s62", "s63", "s64", "s65", "s66", "s67", "s68", "s69", "s70", "s71", "s72", "s73", "s74", "s75", "s76", "s77", "s78", "s79", "s80", "s81", "s82", "s83", "s84", "s85", "s86", "s87", "s88", "s89", "s90", "s91", "s92", "s93", "s94", "s95", "s96", "s97", "s98", "s99", "vcc", "memory");
}

extern "C" void kernel_launch(void* const* d_in, const int* in_sizes, int n_in, void* d_out, int out_size, void* d_ws, size_t ws_size, hipStream_t stream) {
  (void)in_sizes; (void)n_in; (void)out_size; (void)ws_size;
  (void)hipFuncSetAttribute((const void*)mixer_mlp_kernel, hipFuncAttributeMaxDynamicSharedMemorySize, 163840);
  mixer_mlp_kernel<<<256, 256, 163840, stream>>>(d_in[0], d_in[1], d_in[2], d_in[3], d_in[4], d_out, d_ws);
}
```
